```python
import torch

def _softmax(x, axis=-1):
    return torch.softmax(x, dim=axis)
def _gelu(x):
    return torch.nn.functional.gelu(x, approximate="tanh")
def _silu(x):
    return torch.nn.functional.silu(x)
def _sigmoid(x):
    return torch.sigmoid(x)
def _layernorm(x, axis=-1, eps=1e-5):
    m = x.mean(dim=axis, keepdim=True)
    v = x.var(dim=axis, keepdim=True, unbiased=False)
    return (x - m) / torch.sqrt(v + eps)
def _scatter_add(tgt, idx, src, axis=0):
    return tgt.clone().index_add_(axis, idx, src)
def _segment_sum(src, seg, n):
    out = torch.zeros((n,) + src.shape[1:], dtype=src.dtype)
    return out.index_add_(0, seg, src)
def _pad(x, pw):
    flat = tuple(v for pair in reversed(pw) for v in pair)
    return torch.nn.functional.pad(x, flat)

kernel_name = 'concat_heads__sliding_block_cached__compressed_kv_cla__decode'

def setup_inputs():

    _w = (lambda a: (lambda t: t.to(torch.bfloat16) if t.is_floating_point() else t)(torch.as_tensor(a)))
    def _A_setup():
        B, Sq, D = 4, 1, 512
        Hq, Hkv, Sk, Dh = 32, 8, 8192, 128
        assert Hq % Hkv == 0, (Hq, Hkv)
        return {
            "x": _w((lambda *s: torch.randn(*s, dtype=torch.bfloat16) * 1.6)(B, Sq, D)),
            "k_cache": _w((lambda *s: torch.randn(*s, dtype=torch.bfloat16) * 1.6)(B, Hkv, Sk, Dh)),
            "v_cache": _w((lambda *s: torch.randn(*s, dtype=torch.bfloat16) * 0.05)(B, Hkv, Sk, Dh)),
            "wq": _w((lambda *s: torch.randn(*s, dtype=torch.bfloat16) * 1.6)(D, Hq * Dh) * (D ** -0.5)),
            "wo": _w((lambda *s: torch.randn(*s, dtype=torch.bfloat16) * 0.05)(Hq * Dh, D) * ((Hq * Dh) ** -0.5)),
        }
    _A_in = _A_setup()

    def _B_setup():
        B, H, Sq, Sk, D, M = 4, 32, 1, 8192, 128, 512
        Dc = 4 * D
        return {
            "x": _w((lambda *s: torch.randn(*s, dtype=torch.bfloat16) * 3.2)(B, Sq, M)),
            "c_kv": _w((lambda *s: torch.randn(*s, dtype=torch.bfloat16) * 0.1)(B, Sk, Dc)),
            "w_q": _w((lambda *s: torch.randn(*s, dtype=torch.bfloat16) * 6.4)(M, H, D) * (M ** -0.5)),
            "w_uk": _w((lambda *s: torch.randn(*s, dtype=torch.bfloat16) * 6.4)(Dc, H, D) * (Dc ** -0.5)),
            "w_uv": _w((lambda *s: torch.randn(*s, dtype=torch.bfloat16) * 0.1)(Dc, H, D) * (Dc ** -0.5)),
            "w_o": _w((lambda *s: torch.randn(*s, dtype=torch.bfloat16) * 0.1)(H, D, M) * ((H * D) ** -0.5)),
        }
    _B_in = _B_setup()

    return {
        "A_x": _A_in["x"],
        "A_k_cache": _A_in["k_cache"],
        "A_v_cache": _A_in["v_cache"],
        "A_wq": _A_in["wq"],
        "A_wo": _A_in["wo"],
        "B_x": _B_in["x"],
        "B_c_kv": _B_in["c_kv"],
        "B_w_q": _B_in["w_q"],
        "B_w_uk": _B_in["w_uk"],
        "B_w_uv": _B_in["w_uv"],
        "B_w_o": _B_in["w_o"],
    }

def reference(A_x, A_k_cache, A_v_cache, A_wq, A_wo, B_x, B_c_kv, B_w_q, B_w_uk, B_w_uv, B_w_o):
    def _A_ref(x, k_cache, v_cache, wq, wo):
        B, Sq, D = x.shape
        Hq, Dh = 32, 128
        Hkv, Sk = k_cache.shape[1], k_cache.shape[2]
        W = min(4096, max(256, Sk // 4))
        rep = Hq // Hkv
        q = (lambda x, *d: x.permute(*d))((x @ wq).reshape(B, Sq, Hq, Dh), 0, 2, 1, 3)
        k = (lambda x, n, axis: torch.repeat_interleave(x, n, dim=axis))(k_cache[:, :, Sk - W:, :], rep, axis=1)
        v = (lambda x, n, axis: torch.repeat_interleave(x, n, dim=axis))(v_cache[:, :, Sk - W:, :], rep, axis=1)
        scores = torch.einsum("bhqd,bhkd->bhqk", q, k) * (Dh ** -0.5)
        p = _softmax(scores, axis=-1)
        o = torch.einsum("bhqk,bhkd->bhqd", p, v)
        return (lambda x, *d: x.permute(*d))(o, 0, 2, 1, 3).reshape(B, Sq, Hq * Dh) @ wo

    def _B_ref(x, c_kv, w_q, w_uk, w_uv, w_o):
        B, Sq, M = x.shape[0], x.shape[1], x.shape[2]
        Sk = c_kv.shape[1]
        H, D = w_q.shape[1], w_q.shape[2]
        q = torch.einsum("bqm,mhd->bhqd", x, w_q)
        k = torch.einsum("bkc,chd->bhkd", c_kv, w_uk)
        v = torch.einsum("bkc,chd->bhkd", c_kv, w_uv)
        scores = torch.einsum("bhqd,bhkd->bhqk", q, k) * (D ** -0.5)
        off = Sk - Sq
        qi = torch.arange(Sq)[:, None]
        kj = torch.arange(Sk)[None, :]
        cmask = kj <= (qi + off)
        scores = torch.where(cmask[None, None], scores, -1e9)
        p = _softmax(scores, axis=-1)
        ctx = torch.einsum("bhqk,bhkd->bhqd", p, v)
        return torch.einsum("bhqd,hdm->bqm", ctx, w_o)

    a = _A_ref(A_x, A_k_cache, A_v_cache, A_wq, A_wo)
    b = _B_ref(B_x, B_c_kv, B_w_q, B_w_uk, B_w_uv, B_w_o)
    return torch.concatenate([a, b], axis=-1)
```

```cpp
#include <hip/hip_runtime.h>
#include <hip/hip_bf16.h>
#include <stdint.h>

constexpr int NB = 4;
constexpr int SK = 8192;
constexpr int WIN = 2048;
constexpr int NSA = 4;
constexpr int NSB = 32;
constexpr float LOG2E = 1.4426950408889634f;
constexpr float SCALE = 0.08838834764831845f;

constexpr size_t WS_QA   = 0;
constexpr size_t WS_QABS = 65536;
constexpr size_t WS_PAML = WS_QABS + 131072;
constexpr size_t WS_PAO  = WS_PAML + 65536;
constexpr size_t WS_PBML = WS_PAO + 262144 * 2;
constexpr size_t WS_VEC  = WS_PBML + 65536;
constexpr size_t WS_PBC  = WS_VEC + 262144;

typedef __attribute__((ext_vector_type(8))) short bf16x8;
typedef __attribute__((ext_vector_type(4))) short s16x4;
typedef __attribute__((ext_vector_type(16))) float f32x16;
typedef __attribute__((ext_vector_type(4))) float f32x4;
typedef __attribute__((ext_vector_type(4))) unsigned u32x4;
typedef __attribute__((ext_vector_type(2))) __bf16 bf16x2v;
typedef unsigned short u16;

__device__ __forceinline__ float bf2f(u16 v) { return __uint_as_float(((unsigned)v) << 16); }
__device__ __forceinline__ float lo2f(unsigned v) { return __uint_as_float(v << 16); }
__device__ __forceinline__ float hi2f(unsigned v) { return __uint_as_float(v & 0xffff0000u); }
__device__ __forceinline__ unsigned cvtpk(float lo, float hi) {
  unsigned r; asm volatile("v_cvt_pk_bf16_f32 %0, %1, %2" : "=v"(r) : "v"(lo), "v"(hi)); return r;
}
__device__ __forceinline__ u16 f2bf(float x) { return (u16)(cvtpk(x, 0.f) & 0xffffu); }
__device__ __forceinline__ float dot2(unsigned k, unsigned q, float acc) {
  return __builtin_amdgcn_fdot2_f32_bf16(__builtin_bit_cast(bf16x2v, k), __builtin_bit_cast(bf16x2v, q), acc, false);
}

__global__ __launch_bounds__(256) void k1_proj(const u16* __restrict__ xA, const u16* __restrict__ wqA,
                                               const u16* __restrict__ xB, const u16* __restrict__ wqB,
                                               const u16* __restrict__ wuk, char* __restrict__ ws) {
  __shared__ float sx[512 * 4];
  __shared__ float red[64 * 128];
  __shared__ float sq[4 * 128];
  const int t = threadIdx.x;
  const bool isA = blockIdx.x < 128;
  const u16* x = isA ? xA : xB;
  for (int i = t; i < 2048; i += 256) { int b = i >> 9, r = i & 511; sx[r * 4 + b] = bf2f(x[i]); }
  __syncthreads();
  if (isA) {
    const int col0 = blockIdx.x * 32;
    const int tc = t & 3, tr = t >> 2;
    float acc[4][8];
#pragma unroll
    for (int b = 0; b < 4; ++b)
#pragma unroll
      for (int e = 0; e < 8; ++e) acc[b][e] = 0.f;
    u32x4 wv[8];
#pragma unroll
    for (int i = 0; i < 8; ++i) wv[i] = *(const u32x4*)(wqA + (size_t)(tr + 64 * i) * 4096 + col0 + tc * 8);
#pragma unroll
    for (int i = 0; i < 8; ++i) {
      const f32x4 xv = *(const f32x4*)(sx + (tr + 64 * i) * 4);
#pragma unroll
      for (int e = 0; e < 4; ++e) {
        float w0 = lo2f(wv[i][e]), w1 = hi2f(wv[i][e]);
#pragma unroll
        for (int b = 0; b < 4; ++b) { acc[b][2 * e] += xv[b] * w0; acc[b][2 * e + 1] += xv[b] * w1; }
      }
    }
#pragma unroll
    for (int b = 0; b < 4; ++b)
#pragma unroll
      for (int e = 0; e < 8; ++e) red[tr * 128 + b * 32 + tc * 8 + e] = acc[b][e];
    __syncthreads();
    if (t < 128) {
      float s = 0.f;
      for (int i = 0; i < 64; ++i) s += red[i * 128 + t];
      int b = t >> 5, c = t & 31;
      ((u16*)(ws + WS_QA))[b * 4096 + col0 + c] = f2bf(s);
    }
  } else {
    const int id = blockIdx.x - 128, h = id >> 2, cq = id & 3;
    const int tc = t & 15, tr = t >> 4;
    float acc[4][8];
#pragma unroll
    for (int b = 0; b < 4; ++b)
#pragma unroll
      for (int e = 0; e < 8; ++e) acc[b][e] = 0.f;
#pragma unroll
    for (int half = 0; half < 2; ++half) {
      u32x4 wv[16];
#pragma unroll
      for (int i = 0; i < 16; ++i) wv[i] = *(const u32x4*)(wqB + (size_t)(tr + 16 * (i + 16 * half)) * 4096 + h * 128 + tc * 8);
#pragma unroll
      for (int i = 0; i < 16; ++i) {
        const f32x4 xv = *(const f32x4*)(sx + (tr + 16 * (i + 16 * half)) * 4);
#pragma unroll
        for (int e = 0; e < 4; ++e) {
          float w0 = lo2f(wv[i][e]), w1 = hi2f(wv[i][e]);
#pragma unroll
          for (int b = 0; b < 4; ++b) { acc[b][2 * e] += xv[b] * w0; acc[b][2 * e + 1] += xv[b] * w1; }
        }
      }
    }
    u32x4 uk[8];
#pragma unroll
    for (int i = 0; i < 8; ++i) uk[i] = *(const u32x4*)(wuk + ((size_t)(cq * 128 + tr + 16 * i) * 32 + h) * 128 + tc * 8);
#pragma unroll
    for (int b = 0; b < 4; ++b)
#pragma unroll
      for (int e = 0; e < 8; ++e) red[tr * 512 + b * 128 + tc * 8 + e] = acc[b][e];
    __syncthreads();
    for (int o = t; o < 512; o += 256) {
      float s = 0.f;
      for (int i = 0; i < 16; ++i) s += red[i * 512 + o];
      sq[o] = bf2f(f2bf(s));
    }
    __syncthreads();
    float qv[4][8];
#pragma unroll
    for (int b = 0; b < 4; ++b)
#pragma unroll
      for (int e = 0; e < 8; ++e) qv[b][e] = sq[b * 128 + tc * 8 + e];
#pragma unroll
    for (int i = 0; i < 8; ++i) {
      float s[4] = {0.f, 0.f, 0.f, 0.f};
#pragma unroll
      for (int e = 0; e < 4; ++e) {
        float w0 = lo2f(uk[i][e]), w1 = hi2f(uk[i][e]);
#pragma unroll
        for (int b = 0; b < 4; ++b) s[b] += qv[b][2 * e] * w0 + qv[b][2 * e + 1] * w1;
      }
#pragma unroll
      for (int b = 0; b < 4; ++b) {
        s[b] += __shfl_xor(s[b], 1); s[b] += __shfl_xor(s[b], 2); s[b] += __shfl_xor(s[b], 4); s[b] += __shfl_xor(s[b], 8);
      }
      if (tc < 4) {
        float v = (tc == 0) ? s[0] : (tc == 1) ? s[1] : (tc == 2) ? s[2] : s[3];
        int c = cq * 128 + tr + 16 * i;
        ((u16*)(ws + WS_QABS))[(tc * 32 + h) * 512 + c] = f2bf(v * (SCALE * LOG2E));
      }
    }
  }
}

constexpr int K2_LDS = 163840;
__device__ __forceinline__ unsigned off_b(unsigned row, unsigned ch) {
  return 256u * row + 16u * (ch ^ (((row & 3) << 2) | ((row >> 2) & 3)));
}

__device__ __forceinline__ void attnB(int wg, const u16* __restrict__ ckv, char* __restrict__ ws, char* smem) {
  const int b = wg >> 5, split = wg & 31;
  const int tid = threadIdx.x, w = tid >> 6, l = tid & 63;
  const int h5 = l >> 5, r = l & 31;
  char* ring = smem + w * 32768;
  float* sred = (float*)(smem + 131072);
  const u16* qabs = (const u16*)(ws + WS_QABS);
  bf16x8 qf[8];
#pragma unroll
  for (int ks = 0; ks < 8; ++ks) qf[ks] = *(const bf16x8*)(qabs + (b * 32 + r) * 512 + w * 128 + 16 * ks + 8 * h5);
  asm volatile("s_waitcnt vmcnt(0)" ::: "memory");
  const u16* src0 = ckv + ((size_t)b * SK + split * 256 + (l >> 4)) * 512 + w * 128;
  auto issue = [&](int t) {
#pragma unroll
    for (int n = 0; n < 8; ++n) {
      const int ch = (l & 15) ^ (((l >> 4) << 2) | (n & 3));
      const u16* g = src0 + (size_t)(t * 32 + 4 * n) * 512 + ch * 8;
      __builtin_amdgcn_global_load_lds((const unsigned*)g, (unsigned*)(ring + (t & 3) * 8192 + n * 1024), 16, 0, 0);
    }
  };
  issue(0); issue(1); issue(2);
  f32x16 acc[4];
#pragma unroll
  for (int cb = 0; cb < 4; ++cb)
#pragma unroll
    for (int i = 0; i < 16; ++i) acc[cb][i] = 0.f;
  float mrun = -INFINITY, lrun = 0.f;
  unsigned rowoff[8];
#pragma unroll
  for (int ks = 0; ks < 8; ++ks) rowoff[ks] = off_b(r, 2 * ks + h5);
  const unsigned q4 = (l & 15) >> 2, p4 = l & 3, blk = (l >> 4) & 1;

#pragma unroll
  for (int t = 0; t < 8; ++t) {
    if (t + 3 < 8) issue(t + 3);
    {
      constexpr int dummy = 0; (void)dummy;
    }
    if (t + 3 < 8) asm volatile("s_waitcnt vmcnt(24)" ::: "memory");
    else if (t == 5) asm volatile("s_waitcnt vmcnt(16)" ::: "memory");
    else if (t == 6) asm volatile("s_waitcnt vmcnt(8)" ::: "memory");
    else asm volatile("s_waitcnt vmcnt(0)" ::: "memory");
    char* tile = ring + (t & 3) * 8192;
    f32x16 s;
#pragma unroll
    for (int i = 0; i < 16; ++i) s[i] = 0.f;
#pragma unroll
    for (int ks = 0; ks < 8; ++ks) {
      bf16x8 a = *(const bf16x8*)(tile + rowoff[ks]);
      s = __builtin_amdgcn_mfma_f32_32x32x16_bf16(a, qf[ks], s, 0, 0, 0);
    }
    float* sb = sred + (t & 1) * 4096;
#pragma unroll
    for (int g = 0; g < 4; ++g) {
      f32x4 v = {s[4 * g], s[4 * g + 1], s[4 * g + 2], s[4 * g + 3]};
      *(f32x4*)(sb + w * 1024 + (g * 64 + l) * 4) = v;
    }
    asm volatile("s_waitcnt lgkmcnt(0)" ::: "memory");
    __builtin_amdgcn_s_barrier();
#pragma unroll
    for (int g = 0; g < 4; ++g) {
      f32x4 v0 = *(const f32x4*)(sb + 0 * 1024 + (g * 64 + l) * 4);
      f32x4 v1 = *(const f32x4*)(sb + 1 * 1024 + (g * 64 + l) * 4);
      f32x4 v2 = *(const f32x4*)(sb + 2 * 1024 + (g * 64 + l) * 4);
      f32x4 v3 = *(const f32x4*)(sb + 3 * 1024 + (g * 64 + l) * 4);
      f32x4 v = (v0 + v1) + (v2 + v3);
      s[4 * g] = v[0]; s[4 * g + 1] = v[1]; s[4 * g + 2] = v[2]; s[4 * g + 3] = v[3];
    }
    float mloc = s[0];
#pragma unroll
    for (int i = 1; i < 16; ++i) mloc = fmaxf(mloc, s[i]);
    mloc = fmaxf(mloc, __shfl_xor(mloc, 32));
    const float mnew = fmaxf(mrun, mloc);
    const float alpha = exp2f(mrun - mnew);
    mrun = mnew;
    float ps = 0.f;
#pragma unroll
    for (int i = 0; i < 16; ++i) { s[i] = exp2f(s[i] - mnew); ps += s[i]; }
    lrun = lrun * alpha + ps;
    bf16x8 pf[2];
#pragma unroll
    for (int s2 = 0; s2 < 2; ++s2) {
      u32x4 u;
#pragma unroll
      for (int j = 0; j < 4; ++j) u[j] = cvtpk(s[8 * s2 + 2 * j], s[8 * s2 + 2 * j + 1]);
      pf[s2] = __builtin_bit_cast(bf16x8, u);
    }
#pragma unroll
    for (int cb = 0; cb < 4; ++cb)
#pragma unroll
      for (int i = 0; i < 16; ++i) acc[cb][i] *= alpha;
#pragma unroll
    for (int cb = 0; cb < 4; ++cb)
#pragma unroll
      for (int s2 = 0; s2 < 2; ++s2) {
        const unsigned a0 = off_b(16 * s2 + 4 * h5 + q4, 4 * cb + 2 * blk + (p4 >> 1)) + 8 * (p4 & 1);
        const unsigned a1 = off_b(16 * s2 + 8 + 4 * h5 + q4, 4 * cb + 2 * blk + (p4 >> 1)) + 8 * (p4 & 1);
        s16x4 v0 = __builtin_amdgcn_ds_read_tr16_b64_v4i16((__attribute__((address_space(3))) s16x4*)(tile + a0));
        s16x4 v1 = __builtin_amdgcn_ds_read_tr16_b64_v4i16((__attribute__((address_space(3))) s16x4*)(tile + a1));
        bf16x8 a = {v0[0], v0[1], v0[2], v0[3], v1[0], v1[1], v1[2], v1[3]};
        acc[cb] = __builtin_amdgcn_mfma_f32_32x32x16_bf16(a, pf[s2], acc[cb], 0, 0, 0);
      }
  }
  lrun += __shfl_xor(lrun, 32);
  if (w == 0 && l < 32) {
    float* pml = (float*)(ws + WS_PBML) + ((size_t)(b * NSB + split) * 32 + r) * 2;
    pml[0] = mrun; pml[1] = lrun;
  }
  __syncthreads();
  float* tr = (float*)smem;
#pragma unroll
  for (int cb = 0; cb < 4; ++cb)
#pragma unroll
    for (int g = 0; g < 4; ++g) {
      f32x4 v = {acc[cb][4 * g], acc[cb][4 * g + 1], acc[cb][4 * g + 2], acc[cb][4 * g + 3]};
      *(f32x4*)(tr + r * 516 + w * 128 + 32 * cb + 8 * g + 4 * h5) = v;
    }
  __syncthreads();
  float* pc = (float*)(ws + WS_PBC) + (size_t)(b * NSB + split) * 32 * 512;
#pragma unroll
  for (int i = 0; i < 16; ++i) {
    const int idx = (i * 256 + tid) * 4;
    const int hh = idx >> 9, c = idx & 511;
    *(f32x4*)(pc + idx) = *(const f32x4*)(tr + hh * 516 + c);
  }
}

__device__ __forceinline__ void attnA(int wg, const u16* __restrict__ kc, const u16* __restrict__ vc, char* __restrict__ ws, char* smem) {
  const int b = wg >> 5, hkv = (wg >> 2) & 7, split = wg & 3;
  const int tid = threadIdx.x, w = tid >> 6, l = tid & 63;
  const int r4 = l >> 4, cc = l & 15;
  const size_t base = ((size_t)(b * 8 + hkv) * SK + (SK - WIN) + split * 512 + w * 128) * 128;
  const u16* kp = kc + base + (size_t)r4 * 128 + cc * 8;
  const u16* vp = vc + base + (size_t)r4 * 128 + cc * 8;
  const u16* qa = (const u16*)(ws + WS_QA);
  u32x4 qd[4];
#pragma unroll
  for (int g = 0; g < 4; ++g) qd[g] = *(const u32x4*)(qa + b * 4096 + (hkv * 4 + g) * 128 + cc * 8);
  u32x4 kf[8][4];
#pragma unroll
  for (int ch = 0; ch < 8; ++ch)
#pragma unroll
    for (int i = 0; i < 4; ++i) kf[ch][i] = __builtin_nontemporal_load((const u32x4*)(kp + (size_t)(ch * 16 + 4 * i) * 128));
  float sc[8];
#pragma unroll
  for (int ch = 0; ch < 8; ++ch) {
    float v[16];
#pragma unroll
    for (int i = 0; i < 4; ++i)
#pragma unroll
      for (int g = 0; g < 4; ++g) {
        float a = 0.f;
#pragma unroll
        for (int e = 0; e < 4; ++e) a = dot2(kf[ch][i][e], qd[g][e], a);
        v[i * 4 + g] = a;
      }
    float v8[8], v4[4], v2[2];
#pragma unroll
    for (int k = 0; k < 8; ++k) {
      float keep = (cc & 8) ? v[k + 8] : v[k], send = (cc & 8) ? v[k] : v[k + 8];
      v8[k] = keep + __shfl_xor(send, 8);
    }
#pragma unroll
    for (int k = 0; k < 4; ++k) {
      float keep = (cc & 4) ? v8[k + 4] : v8[k], send = (cc & 4) ? v8[k] : v8[k + 4];
      v4[k] = keep + __shfl_xor(send, 4);
    }
#pragma unroll
    for (int k = 0; k < 2; ++k) {
      float keep = (cc & 2) ? v4[k + 2] : v4[k], send = (cc & 2) ? v4[k] : v4[k + 2];
      v2[k] = keep + __shfl_xor(send, 2);
    }
    {
      float keep = (cc & 1) ? v2[1] : v2[0], send = (cc & 1) ? v2[0] : v2[1];
      sc[ch] = (keep + __shfl_xor(send, 1)) * (SCALE * LOG2E);
    }
  }
  u32x4 vf[8][4];
#pragma unroll
  for (int ch = 0; ch < 8; ++ch)
#pragma unroll
    for (int i = 0; i < 4; ++i) vf[ch][i] = __builtin_nontemporal_load((const u32x4*)(vp + (size_t)(ch * 16 + 4 * i) * 128));
  float m = sc[0];
#pragma unroll
  for (int ch = 1; ch < 8; ++ch) m = fmaxf(m, sc[ch]);
  m = fmaxf(m, __shfl_xor(m, 4)); m = fmaxf(m, __shfl_xor(m, 8)); m = fmaxf(m, __shfl_xor(m, 16)); m = fmaxf(m, __shfl_xor(m, 32));
  float ls = 0.f;
#pragma unroll
  for (int ch = 0; ch < 8; ++ch) { sc[ch] = exp2f(sc[ch] - m); ls += sc[ch]; }
  ls += __shfl_xor(ls, 4); ls += __shfl_xor(ls, 8); ls += __shfl_xor(ls, 16); ls += __shfl_xor(ls, 32);
  float acc[4][8];
#pragma unroll
  for (int g = 0; g < 4; ++g)
#pragma unroll
    for (int e = 0; e < 8; ++e) acc[g][e] = 0.f;
#pragma unroll
  for (int ch = 0; ch < 8; ++ch) {
#pragma unroll
    for (int i = 0; i < 4; ++i) {
      float vv[8];
#pragma unroll
      for (int e = 0; e < 4; ++e) { vv[2 * e] = lo2f(vf[ch][i][e]); vv[2 * e + 1] = hi2f(vf[ch][i][e]); }
#pragma unroll
      for (int g = 0; g < 4; ++g) {
        const float p = __shfl(sc[ch], (l & 48) + 4 * i + g);
#pragma unroll
        for (int e = 0; e < 8; ++e) acc[g][e] += p * vv[e];
      }
    }
  }
#pragma unroll
  for (int g = 0; g < 4; ++g)
#pragma unroll
    for (int e = 0; e < 8; ++e) { acc[g][e] += __shfl_xor(acc[g][e], 16); acc[g][e] += __shfl_xor(acc[g][e], 32); }
  float* sm = (float*)smem;
  float* so = sm + 64;
  if (l < 4) { sm[(w * 4 + l) * 2] = m; sm[(w * 4 + l) * 2 + 1] = ls; }
  if (r4 == 0) {
#pragma unroll
    for (int g = 0; g < 4; ++g)
#pragma unroll
      for (int e = 0; e < 8; ++e) so[(w * 4 + g) * 128 + cc * 8 + e] = acc[g][e];
  }
  __syncthreads();
  for (int o = tid; o < 512; o += 256) {
    const int g = o >> 7, d = o & 127;
    float M = -INFINITY;
#pragma unroll
    for (int ww = 0; ww < 4; ++ww) M = fmaxf(M, sm[(ww * 4 + g) * 2]);
    float L = 0.f, O = 0.f;
#pragma unroll
    for (int ww = 0; ww < 4; ++ww) {
      const float f = exp2f(sm[(ww * 4 + g) * 2] - M);
      L += f * sm[(ww * 4 + g) * 2 + 1];
      O += f * so[(ww * 4 + g) * 128 + d];
    }
    const int hq = hkv * 4 + g;
    ((float*)(ws + WS_PAO))[((size_t)(b * 32 + hq) * NSA + split) * 128 + d] = O;
    if (d == 0) {
      float* pml = (float*)(ws + WS_PAML) + ((size_t)(b * 32 + hq) * NSA + split) * 2;
      pml[0] = M; pml[1] = L;
    }
  }
}

__global__ __launch_bounds__(256, 1) void k2_attn(const u16* __restrict__ kc, const u16* __restrict__ vc,
                                                  const u16* __restrict__ ckv, char* __restrict__ ws) {
  extern __shared__ __attribute__((aligned(16))) char smem[];
  if (blockIdx.x < 128) attnB(blockIdx.x, ckv, ws, smem);
  else attnA(blockIdx.x - 128, kc, vc, ws, smem);
}

__global__ __launch_bounds__(256) void k3_combine(const u16* __restrict__ wuv, char* __restrict__ ws) {
  __shared__ float sw[32];
  __shared__ float sc[512];
  __shared__ float red[32 * 64];
  const int t = threadIdx.x;
  float* vec = (float*)(ws + WS_VEC);
  if (blockIdx.x >= 256) {
    const float* pml = (const float*)(ws + WS_PAML);
    const float* po = (const float*)(ws + WS_PAO);
    for (int o = (blockIdx.x - 256) * 256 + t; o < 16384; o += 4096) {
      const int bh = o >> 7, d = o & 127;
      float M = -INFINITY;
#pragma unroll
      for (int s = 0; s < NSA; ++s) M = fmaxf(M, pml[(bh * NSA + s) * 2]);
      float L = 0.f, O = 0.f;
#pragma unroll
      for (int s = 0; s < NSA; ++s) {
        const float f = exp2f(pml[(bh * NSA + s) * 2] - M);
        L += f * pml[(bh * NSA + s) * 2 + 1];
        O += f * po[(bh * NSA + s) * 128 + d];
      }
      vec[o] = bf2f(f2bf(O / L));
    }
    return;
  }
  const int h = blockIdx.x >> 3, b = (blockIdx.x >> 1) & 3, dh = blockIdx.x & 1;
  const int tc = t & 7, tr = t >> 3;
  u32x4 wv[16];
#pragma unroll
  for (int i = 0; i < 16; ++i) wv[i] = *(const u32x4*)(wuv + ((size_t)(tr + 32 * i) * 32 + h) * 128 + dh * 64 + tc * 8);
  const float* pml = (const float*)(ws + WS_PBML);
  if (t < 64) {
    float mm = (t < 32) ? pml[((b * NSB + t) * 32 + h) * 2] : -INFINITY;
    float ll = (t < 32) ? pml[((b * NSB + t) * 32 + h) * 2 + 1] : 0.f;
    float M = mm;
#pragma unroll
    for (int o = 1; o < 64; o <<= 1) M = fmaxf(M, __shfl_xor(M, o));
    float f = exp2f(mm - M);
    float L = f * ll;
#pragma unroll
    for (int o = 1; o < 64; o <<= 1) L += __shfl_xor(L, o);
    if (t < 32) sw[t] = f / L;
  }
  __syncthreads();
  {
    const float* pc = (const float*)(ws + WS_PBC) + ((size_t)b * NSB * 32 + h) * 512 + 2 * t;
    float a0 = 0.f, a1 = 0.f;
#pragma unroll
    for (int s = 0; s < NSB; ++s) {
      const float2 v = *(const float2*)(pc + (size_t)s * 32 * 512);
      a0 += sw[s] * v.x; a1 += sw[s] * v.y;
    }
    sc[2 * t] = a0; sc[2 * t + 1] = a1;
  }
  __syncthreads();
  float acc[8];
#pragma unroll
  for (int e = 0; e < 8; ++e) acc[e] = 0.f;
#pragma unroll
  for (int i = 0; i < 16; ++i) {
    const float cv = sc[tr + 32 * i];
#pragma unroll
    for (int e = 0; e < 4; ++e) { acc[2 * e] += cv * lo2f(wv[i][e]); acc[2 * e + 1] += cv * hi2f(wv[i][e]); }
  }
#pragma unroll
  for (int e = 0; e < 8; ++e) red[tr * 64 + tc * 8 + e] = acc[e];
  __syncthreads();
  if (t < 64) {
    float s = 0.f;
    for (int i = 0; i < 32; ++i) s += red[i * 64 + t];
    vec[16384 + b * 4096 + h * 128 + dh * 64 + t] = bf2f(f2bf(s));
  }
}

__global__ __launch_bounds__(256) void k4_out(const u16* __restrict__ woA, const u16* __restrict__ woB,
                                              const char* __restrict__ ws, u16* __restrict__ out) {
  __shared__ float red[4][32];
  const int t = threadIdx.x, part = blockIdx.x >> 6, col0 = (blockIdx.x & 63) * 8;
  const u16* wo = part ? woB : woA;
  const float* vec = (const float*)(ws + WS_VEC) + part * 16384;
  u32x4 wv[16];
#pragma unroll
  for (int i = 0; i < 16; ++i) wv[i] = *(const u32x4*)(wo + (size_t)(t + 256 * i) * 512 + col0);
  float acc[4][8];
#pragma unroll
  for (int b = 0; b < 4; ++b)
#pragma unroll
    for (int e = 0; e < 8; ++e) acc[b][e] = 0.f;
#pragma unroll
  for (int i = 0; i < 16; ++i) {
    float xv[4];
#pragma unroll
    for (int b = 0; b < 4; ++b) xv[b] = vec[b * 4096 + t + 256 * i];
#pragma unroll
    for (int e = 0; e < 4; ++e) {
      const float w0 = lo2f(wv[i][e]), w1 = hi2f(wv[i][e]);
#pragma unroll
      for (int b = 0; b < 4; ++b) { acc[b][2 * e] += xv[b] * w0; acc[b][2 * e + 1] += xv[b] * w1; }
    }
  }
#pragma unroll
  for (int b = 0; b < 4; ++b)
#pragma unroll
    for (int e = 0; e < 8; ++e) {
      float v = acc[b][e];
#pragma unroll
      for (int o = 1; o < 64; o <<= 1) v += __shfl_xor(v, o);
      acc[b][e] = v;
    }
  if ((t & 63) == 0) {
#pragma unroll
    for (int b = 0; b < 4; ++b)
#pragma unroll
      for (int e = 0; e < 8; ++e) red[t >> 6][b * 8 + e] = acc[b][e];
  }
  __syncthreads();
  if (t < 32) {
    const float s = (red[0][t] + red[1][t]) + (red[2][t] + red[3][t]);
    const int b = t >> 3, e = t & 7;
    out[b * 1024 + part * 512 + col0 + e] = f2bf(s);
  }
}

extern "C" void kernel_launch(void* const* d_in, const int* in_sizes, int n_in,
                              void* d_out, int out_size, void* d_ws, size_t ws_size,
                              hipStream_t stream) {
  const u16* xA  = (const u16*)d_in[0];
  const u16* kc  = (const u16*)d_in[1];
  const u16* vc  = (const u16*)d_in[2];
  const u16* wqA = (const u16*)d_in[3];
  const u16* woA = (const u16*)d_in[4];
  const u16* xB  = (const u16*)d_in[5];
  const u16* ckv = (const u16*)d_in[6];
  const u16* wqB = (const u16*)d_in[7];
  const u16* wuk = (const u16*)d_in[8];
  const u16* wuv = (const u16*)d_in[9];
  const u16* woB = (const u16*)d_in[10];
  char* ws = (char*)d_ws;
  hipFuncSetAttribute((const void*)k2_attn, hipFuncAttributeMaxDynamicSharedMemorySize, K2_LDS);
  k1_proj<<<256, 256, 0, stream>>>(xA, wqA, xB, wqB, wuk, ws);
  k2_attn<<<256, 256, K2_LDS, stream>>>(kc, vc, ckv, ws);
  k3_combine<<<272, 256, 0, stream>>>(wuv, ws);
  k4_out<<<128, 256, 0, stream>>>(woA, woB, ws, (u16*)d_out);
}
```

```cpp
#include <hip/hip_runtime.h>
#include <hip/hip_bf16.h>
#include <stdint.h>

constexpr int NB = 4;
constexpr int SK = 8192;
constexpr int WIN = 2048;
constexpr int NSA = 4;
constexpr int NSB = 32;
constexpr float LOG2E = 1.4426950408889634f;
constexpr float SCALE = 0.08838834764831845f;

constexpr size_t WS_QA   = 0;
constexpr size_t WS_QABS = 65536;
constexpr size_t WS_PAML = WS_QABS + 131072;
constexpr size_t WS_PAO  = WS_PAML + 65536;
constexpr size_t WS_PBML = WS_PAO + 262144 * 2;
constexpr size_t WS_VEC  = WS_PBML + 65536;
constexpr size_t WS_QB   = WS_VEC + 262144;
constexpr size_t WS_SLAB = WS_QB + 65536;
constexpr size_t WS_PBC  = WS_SLAB + 2097152;

typedef __attribute__((ext_vector_type(8))) short bf16x8;
typedef __attribute__((ext_vector_type(4))) short s16x4;
typedef __attribute__((ext_vector_type(16))) float f32x16;
typedef __attribute__((ext_vector_type(4))) float f32x4;
typedef __attribute__((ext_vector_type(4))) unsigned u32x4;
typedef __attribute__((ext_vector_type(2))) __bf16 bf16x2v;
typedef unsigned short u16;

__device__ __forceinline__ float bf2f(u16 v) { return __uint_as_float(((unsigned)v) << 16); }
__device__ __forceinline__ float lo2f(unsigned v) { return __uint_as_float(v << 16); }
__device__ __forceinline__ float hi2f(unsigned v) { return __uint_as_float(v & 0xffff0000u); }
__device__ __forceinline__ unsigned cvtpk(float lo, float hi) {
  unsigned r; asm volatile("v_cvt_pk_bf16_f32 %0, %1, %2" : "=v"(r) : "v"(lo), "v"(hi)); return r;
}
__device__ __forceinline__ u16 f2bf(float x) { return (u16)(cvtpk(x, 0.f) & 0xffffu); }
template <typename T> __device__ __forceinline__ T ldnt(const void* p) { return __builtin_nontemporal_load((const T*)p); }
__device__ __forceinline__ float dot2(unsigned k, unsigned q, float acc) {
  return __builtin_amdgcn_fdot2_f32_bf16(__builtin_bit_cast(bf16x2v, k), __builtin_bit_cast(bf16x2v, q), acc, false);
}

__global__ __launch_bounds__(256) void k1_proj(const u16* __restrict__ xA, const u16* __restrict__ wqA,
                                               const u16* __restrict__ xB, const u16* __restrict__ wqB,
                                               char* __restrict__ ws) {
  __shared__ float sx[512 * 4];
  __shared__ float red[64 * 128];
  const int t = threadIdx.x;
  const bool isA = blockIdx.x < 128;
  const u16* x = isA ? xA : xB;
  const u16* wq = isA ? wqA : wqB;
  u16* dst = (u16*)(ws + (isA ? WS_QA : WS_QB));
  const int col0 = (blockIdx.x & 127) * 32;
  const int tc = t & 3, tr = t >> 2;
  u32x4 wv[8];
#pragma unroll
  for (int i = 0; i < 8; ++i) wv[i] = ldnt<u32x4>(wq + (size_t)(tr + 64 * i) * 4096 + col0 + tc * 8);
  for (int i = t; i < 2048; i += 256) { int b = i >> 9, r = i & 511; sx[r * 4 + b] = bf2f(x[i]); }
  __syncthreads();
  float acc[4][8];
#pragma unroll
  for (int b = 0; b < 4; ++b)
#pragma unroll
    for (int e = 0; e < 8; ++e) acc[b][e] = 0.f;
#pragma unroll
  for (int i = 0; i < 8; ++i) {
    const f32x4 xv = *(const f32x4*)(sx + (tr + 64 * i) * 4);
#pragma unroll
    for (int e = 0; e < 4; ++e) {
      float w0 = lo2f(wv[i][e]), w1 = hi2f(wv[i][e]);
#pragma unroll
      for (int b = 0; b < 4; ++b) { acc[b][2 * e] += xv[b] * w0; acc[b][2 * e + 1] += xv[b] * w1; }
    }
  }
#pragma unroll
  for (int b = 0; b < 4; ++b)
#pragma unroll
    for (int e = 0; e < 8; ++e) red[tr * 128 + b * 32 + tc * 8 + e] = acc[b][e];
  __syncthreads();
  if (t < 128) {
    float s = 0.f;
    for (int i = 0; i < 64; ++i) s += red[i * 128 + t];
    int b = t >> 5, c = t & 31;
    dst[b * 4096 + col0 + c] = f2bf(s);
  }
}

__global__ __launch_bounds__(256) void k_abs(const u16* __restrict__ wuk, char* __restrict__ ws) {
  const int t = threadIdx.x, h = blockIdx.x >> 3, c0 = (blockIdx.x & 7) * 64;
  const int tc = t & 15, tr = t >> 4;
  u32x4 uk[4];
#pragma unroll
  for (int i = 0; i < 4; ++i) uk[i] = ldnt<u32x4>(wuk + ((size_t)(c0 + tr + 16 * i) * 32 + h) * 128 + tc * 8);
  const u16* qb = (const u16*)(ws + WS_QB);
  float qv[4][8];
#pragma unroll
  for (int b = 0; b < 4; ++b) {
    u32x4 q = *(const u32x4*)(qb + b * 4096 + h * 128 + tc * 8);
#pragma unroll
    for (int e = 0; e < 4; ++e) { qv[b][2 * e] = lo2f(q[e]); qv[b][2 * e + 1] = hi2f(q[e]); }
  }
#pragma unroll
  for (int i = 0; i < 4; ++i) {
    float s[4] = {0.f, 0.f, 0.f, 0.f};
#pragma unroll
    for (int e = 0; e < 4; ++e) {
      float w0 = lo2f(uk[i][e]), w1 = hi2f(uk[i][e]);
#pragma unroll
      for (int b = 0; b < 4; ++b) s[b] += qv[b][2 * e] * w0 + qv[b][2 * e + 1] * w1;
    }
#pragma unroll
    for (int b = 0; b < 4; ++b) {
      s[b] += __shfl_xor(s[b], 1); s[b] += __shfl_xor(s[b], 2); s[b] += __shfl_xor(s[b], 4); s[b] += __shfl_xor(s[b], 8);
    }
    if (tc < 4) {
      float v = (tc == 0) ? s[0] : (tc == 1) ? s[1] : (tc == 2) ? s[2] : s[3];
      int c = c0 + tr + 16 * i;
      ((u16*)(ws + WS_QABS))[(tc * 32 + h) * 512 + c] = f2bf(v * (SCALE * LOG2E));
    }
  }
}

constexpr int K2_LDS = 163840;
__device__ __forceinline__ unsigned off_b(unsigned row, unsigned ch) {
  return 256u * row + 16u * (ch ^ (((row & 3) << 2) | ((row >> 2) & 3)));
}

__device__ __forceinline__ void attnB(int wg, const u16* __restrict__ ckv, char* __restrict__ ws, char* smem) {
  const int b = wg >> 5, split = wg & 31;
  const int tid = threadIdx.x, w = tid >> 6, l = tid & 63;
  const int h5 = l >> 5, r = l & 31;
  char* ring = smem + w * 32768;
  float* sred = (float*)(smem + 131072);
  const u16* qabs = (const u16*)(ws + WS_QABS);
  char* qlds = smem + 131072 + w * 8192;
#pragma unroll
  for (int n = 0; n < 8; ++n) {
    const int row = 4 * n + (l >> 4);
    const int ch = (l & 15) ^ (((l >> 4) << 2) | (n & 3));
    const u16* g = qabs + (b * 32 + row) * 512 + w * 128 + ch * 8;
    __builtin_amdgcn_global_load_lds((const unsigned*)g, (unsigned*)(qlds + n * 1024), 16, 0, 0);
  }
  const u16* src0 = ckv + ((size_t)b * SK + split * 256 + (l >> 4)) * 512 + w * 128;
  auto issue = [&](int t) {
#pragma unroll
    for (int n = 0; n < 8; ++n) {
      const int ch = (l & 15) ^ (((l >> 4) << 2) | (n & 3));
      const u16* g = src0 + (size_t)(t * 32 + 4 * n) * 512 + ch * 8;
      __builtin_amdgcn_global_load_lds((const unsigned*)g, (unsigned*)(ring + (t & 3) * 8192 + n * 1024), 16, 0, 2);
    }
  };
  issue(0); issue(1); issue(2); issue(3);
  asm volatile("s_waitcnt vmcnt(32)" ::: "memory");
  bf16x8 qf[8];
#pragma unroll
  for (int ks = 0; ks < 8; ++ks) qf[ks] = *(const bf16x8*)(qlds + off_b(r, 2 * ks + h5));
  asm volatile("s_waitcnt lgkmcnt(0)" ::: "memory");
  __builtin_amdgcn_s_barrier();
  f32x16 acc[4];
#pragma unroll
  for (int cb = 0; cb < 4; ++cb)
#pragma unroll
    for (int i = 0; i < 16; ++i) acc[cb][i] = 0.f;
  float mrun = -INFINITY, lrun = 0.f;
  unsigned rowoff[8];
#pragma unroll
  for (int ks = 0; ks < 8; ++ks) rowoff[ks] = off_b(r, 2 * ks + h5);
  const unsigned q4 = (l & 15) >> 2, p4 = l & 3, blk = (l >> 4) & 1;
#pragma unroll
  for (int it = 0; it < 4; ++it) {
    if (it < 3) asm volatile("s_waitcnt vmcnt(16)" ::: "memory");
    else asm volatile("s_waitcnt vmcnt(0)" ::: "memory");
    char* tile0 = ring + ((2 * it) & 3) * 8192;
    char* tile1 = ring + ((2 * it + 1) & 3) * 8192;
    f32x16 sa, sb16;
#pragma unroll
    for (int i = 0; i < 16; ++i) { sa[i] = 0.f; sb16[i] = 0.f; }
#pragma unroll
    for (int ks = 0; ks < 8; ++ks) {
      bf16x8 a0 = *(const bf16x8*)(tile0 + rowoff[ks]);
      bf16x8 a1 = *(const bf16x8*)(tile1 + rowoff[ks]);
      sa = __builtin_amdgcn_mfma_f32_32x32x16_bf16(a0, qf[ks], sa, 0, 0, 0);
      sb16 = __builtin_amdgcn_mfma_f32_32x32x16_bf16(a1, qf[ks], sb16, 0, 0, 0);
    }
    float* sbuf = sred;
#pragma unroll
    for (int g = 0; g < 4; ++g) {
      f32x4 v = {sa[4 * g], sa[4 * g + 1], sa[4 * g + 2], sa[4 * g + 3]};
      *(f32x4*)(sbuf + w * 1024 + (g * 64 + l) * 4) = v;
    }
    f32x4 vb[4];
#pragma unroll
    for (int g = 0; g < 4; ++g) {
      f32x4 v = {sb16[4 * g], sb16[4 * g + 1], sb16[4 * g + 2], sb16[4 * g + 3]};
      *(f32x4*)(sbuf + 4096 + w * 1024 + (g * 64 + l) * 4) = v;
      (void)vb;
    }
    asm volatile("s_waitcnt lgkmcnt(0)" ::: "memory");
    __builtin_amdgcn_s_barrier();
    float s2[32];
#pragma unroll
    for (int tt = 0; tt < 2; ++tt)
#pragma unroll
      for (int g = 0; g < 4; ++g) {
        const float* base2 = sbuf + tt * 4096;
        f32x4 v0 = *(const f32x4*)(base2 + 0 * 1024 + (g * 64 + l) * 4);
        f32x4 v1 = *(const f32x4*)(base2 + 1 * 1024 + (g * 64 + l) * 4);
        f32x4 v2 = *(const f32x4*)(base2 + 2 * 1024 + (g * 64 + l) * 4);
        f32x4 v3 = *(const f32x4*)(base2 + 3 * 1024 + (g * 64 + l) * 4);
        f32x4 v = (v0 + v1) + (v2 + v3);
        s2[16 * tt + 4 * g] = v[0]; s2[16 * tt + 4 * g + 1] = v[1]; s2[16 * tt + 4 * g + 2] = v[2]; s2[16 * tt + 4 * g + 3] = v[3];
      }
    asm volatile("s_waitcnt lgkmcnt(0)" ::: "memory");
    __builtin_amdgcn_s_barrier();
    float mloc = s2[0];
#pragma unroll
    for (int i = 1; i < 32; ++i) mloc = fmaxf(mloc, s2[i]);
    mloc = fmaxf(mloc, __shfl_xor(mloc, 32));
    const float mnew = fmaxf(mrun, mloc);
    const float alpha = exp2f(mrun - mnew);
    mrun = mnew;
    float ps = 0.f;
#pragma unroll
    for (int i = 0; i < 32; ++i) { s2[i] = exp2f(s2[i] - mnew); ps += s2[i]; }
    lrun = lrun * alpha + ps;
    bf16x8 pf[4];
#pragma unroll
    for (int sq = 0; sq < 4; ++sq) {
      u32x4 u;
#pragma unroll
      for (int j = 0; j < 4; ++j) u[j] = cvtpk(s2[8 * sq + 2 * j], s2[8 * sq + 2 * j + 1]);
      pf[sq] = __builtin_bit_cast(bf16x8, u);
    }
#pragma unroll
    for (int cb = 0; cb < 4; ++cb)
#pragma unroll
      for (int i = 0; i < 16; ++i) acc[cb][i] *= alpha;
#pragma unroll
    for (int tt = 0; tt < 2; ++tt) {
      const char* tile = tt ? tile1 : tile0;
#pragma unroll
      for (int cb = 0; cb < 4; ++cb)
#pragma unroll
        for (int sk = 0; sk < 2; ++sk) {
          const unsigned a0 = off_b(16 * sk + 4 * h5 + q4, 4 * cb + 2 * blk + (p4 >> 1)) + 8 * (p4 & 1);
          const unsigned a1 = off_b(16 * sk + 8 + 4 * h5 + q4, 4 * cb + 2 * blk + (p4 >> 1)) + 8 * (p4 & 1);
          s16x4 v0 = __builtin_amdgcn_ds_read_tr16_b64_v4i16((__attribute__((address_space(3))) s16x4*)(tile + a0));
          s16x4 v1 = __builtin_amdgcn_ds_read_tr16_b64_v4i16((__attribute__((address_space(3))) s16x4*)(tile + a1));
          bf16x8 a = {v0[0], v0[1], v0[2], v0[3], v1[0], v1[1], v1[2], v1[3]};
          acc[cb] = __builtin_amdgcn_mfma_f32_32x32x16_bf16(a, pf[2 * tt + sk], acc[cb], 0, 0, 0);
        }
    }
    if (it < 2) { asm volatile("s_waitcnt lgkmcnt(0)" ::: "memory"); issue(2 * it + 4); issue(2 * it + 5); }
  }
  lrun += __shfl_xor(lrun, 32);
  if (w == 0 && l < 32) {
    float* pml = (float*)(ws + WS_PBML) + ((size_t)(b * NSB + split) * 32 + r) * 2;
    pml[0] = mrun; pml[1] = lrun;
  }
  __syncthreads();
  float* tr = (float*)smem;
#pragma unroll
  for (int cb = 0; cb < 4; ++cb)
#pragma unroll
    for (int g = 0; g < 4; ++g) {
      f32x4 v = {acc[cb][4 * g], acc[cb][4 * g + 1], acc[cb][4 * g + 2], acc[cb][4 * g + 3]};
      *(f32x4*)(tr + r * 516 + w * 128 + 32 * cb + 8 * g + 4 * h5) = v;
    }
  __syncthreads();
  float* pc = (float*)(ws + WS_PBC) + (size_t)(b * NSB + split) * 32 * 512;
#pragma unroll
  for (int i = 0; i < 16; ++i) {
    const int idx = (i * 256 + tid) * 4;
    const int hh = idx >> 9, c = idx & 511;
    *(f32x4*)(pc + idx) = *(const f32x4*)(tr + hh * 516 + c);
  }
}

__device__ __forceinline__ void attnA(int wg, const u16* __restrict__ kc, const u16* __restrict__ vc, char* __restrict__ ws, char* smem) {
  const int b = wg >> 5, hkv = (wg >> 2) & 7, split = wg & 3;
  const int tid = threadIdx.x, w = tid >> 6, l = tid & 63;
  const int r4 = l >> 4, cc = l & 15;
  const size_t base = ((size_t)(b * 8 + hkv) * SK + (SK - WIN) + split * 512 + w * 128) * 128;
  const u16* kp = kc + base + (size_t)r4 * 128 + cc * 8;
  const u16* vp = vc + base + (size_t)r4 * 128 + cc * 8;
  const u16* qa = (const u16*)(ws + WS_QA);
  u32x4 qd[4];
#pragma unroll
  for (int g = 0; g < 4; ++g) qd[g] = *(const u32x4*)(qa + b * 4096 + (hkv * 4 + g) * 128 + cc * 8);
  u32x4 kf[8][4];
#pragma unroll
  for (int ch = 0; ch < 8; ++ch)
#pragma unroll
    for (int i = 0; i < 4; ++i) kf[ch][i] = __builtin_nontemporal_load((const u32x4*)(kp + (size_t)(ch * 16 + 4 * i) * 128));
  float sc[8];
#pragma unroll
  for (int ch = 0; ch < 8; ++ch) {
    float v[16];
#pragma unroll
    for (int i = 0; i < 4; ++i)
#pragma unroll
      for (int g = 0; g < 4; ++g) {
        float a = 0.f;
#pragma unroll
        for (int e = 0; e < 4; ++e) a = dot2(kf[ch][i][e], qd[g][e], a);
        v[i * 4 + g] = a;
      }
    float v8[8], v4[4], v2[2];
#pragma unroll
    for (int k = 0; k < 8; ++k) {
      float keep = (cc & 8) ? v[k + 8] : v[k], send = (cc & 8) ? v[k] : v[k + 8];
      v8[k] = keep + __shfl_xor(send, 8);
    }
#pragma unroll
    for (int k = 0; k < 4; ++k) {
      float keep = (cc & 4) ? v8[k + 4] : v8[k], send = (cc & 4) ? v8[k] : v8[k + 4];
      v4[k] = keep + __shfl_xor(send, 4);
    }
#pragma unroll
    for (int k = 0; k < 2; ++k) {
      float keep = (cc & 2) ? v4[k + 2] : v4[k], send = (cc & 2) ? v4[k] : v4[k + 2];
      v2[k] = keep + __shfl_xor(send, 2);
    }
    {
      float keep = (cc & 1) ? v2[1] : v2[0], send = (cc & 1) ? v2[0] : v2[1];
      sc[ch] = (keep + __shfl_xor(send, 1)) * (SCALE * LOG2E);
    }
  }
  u32x4 vf[8][4];
#pragma unroll
  for (int ch = 0; ch < 8; ++ch)
#pragma unroll
    for (int i = 0; i < 4; ++i) vf[ch][i] = __builtin_nontemporal_load((const u32x4*)(vp + (size_t)(ch * 16 + 4 * i) * 128));
  float m = sc[0];
#pragma unroll
  for (int ch = 1; ch < 8; ++ch) m = fmaxf(m, sc[ch]);
  m = fmaxf(m, __shfl_xor(m, 4)); m = fmaxf(m, __shfl_xor(m, 8)); m = fmaxf(m, __shfl_xor(m, 16)); m = fmaxf(m, __shfl_xor(m, 32));
  float ls = 0.f;
#pragma unroll
  for (int ch = 0; ch < 8; ++ch) { sc[ch] = exp2f(sc[ch] - m); ls += sc[ch]; }
  ls += __shfl_xor(ls, 4); ls += __shfl_xor(ls, 8); ls += __shfl_xor(ls, 16); ls += __shfl_xor(ls, 32);
  float acc[4][8];
#pragma unroll
  for (int g = 0; g < 4; ++g)
#pragma unroll
    for (int e = 0; e < 8; ++e) acc[g][e] = 0.f;
#pragma unroll
  for (int ch = 0; ch < 8; ++ch) {
#pragma unroll
    for (int i = 0; i < 4; ++i) {
      float vv[8];
#pragma unroll
      for (int e = 0; e < 4; ++e) { vv[2 * e] = lo2f(vf[ch][i][e]); vv[2 * e + 1] = hi2f(vf[ch][i][e]); }
#pragma unroll
      for (int g = 0; g < 4; ++g) {
        const float p = __shfl(sc[ch], (l & 48) + 4 * i + g);
#pragma unroll
        for (int e = 0; e < 8; ++e) acc[g][e] += p * vv[e];
      }
    }
  }
#pragma unroll
  for (int g = 0; g < 4; ++g)
#pragma unroll
    for (int e = 0; e < 8; ++e) { acc[g][e] += __shfl_xor(acc[g][e], 16); acc[g][e] += __shfl_xor(acc[g][e], 32); }
  float* sm = (float*)smem;
  float* so = sm + 64;
  if (l < 4) { sm[(w * 4 + l) * 2] = m; sm[(w * 4 + l) * 2 + 1] = ls; }
  if (r4 == 0) {
#pragma unroll
    for (int g = 0; g < 4; ++g)
#pragma unroll
      for (int e = 0; e < 8; ++e) so[(w * 4 + g) * 128 + cc * 8 + e] = acc[g][e];
  }
  __syncthreads();
  for (int o = tid; o < 512; o += 256) {
    const int g = o >> 7, d = o & 127;
    float M = -INFINITY;
#pragma unroll
    for (int ww = 0; ww < 4; ++ww) M = fmaxf(M, sm[(ww * 4 + g) * 2]);
    float L = 0.f, O = 0.f;
#pragma unroll
    for (int ww = 0; ww < 4; ++ww) {
      const float f = exp2f(sm[(ww * 4 + g) * 2] - M);
      L += f * sm[(ww * 4 + g) * 2 + 1];
      O += f * so[(ww * 4 + g) * 128 + d];
    }
    const int hq = hkv * 4 + g;
    ((float*)(ws + WS_PAO))[((size_t)(b * 32 + hq) * NSA + split) * 128 + d] = O;
    if (d == 0) {
      float* pml = (float*)(ws + WS_PAML) + ((size_t)(b * 32 + hq) * NSA + split) * 2;
      pml[0] = M; pml[1] = L;
    }
  }
}

__global__ __launch_bounds__(256, 1) void k2_attn(const u16* __restrict__ kc, const u16* __restrict__ vc,
                                                  const u16* __restrict__ ckv, char* __restrict__ ws) {
  extern __shared__ __attribute__((aligned(16))) char smem[];
  if (blockIdx.x < 128) attnB(blockIdx.x, ckv, ws, smem);
  else attnA(blockIdx.x - 128, kc, vc, ws, smem);
}

__global__ __launch_bounds__(256) void k3_combine(const u16* __restrict__ wuv, char* __restrict__ ws) {
  __shared__ float sw[32];
  __shared__ float sc[512];
  __shared__ float red[32 * 64];
  const int t = threadIdx.x;
  float* vec = (float*)(ws + WS_VEC);
  if (blockIdx.x >= 256) {
    const float* pml = (const float*)(ws + WS_PAML);
    const float* po = (const float*)(ws + WS_PAO);
    for (int o = (blockIdx.x - 256) * 256 + t; o < 16384; o += 4096) {
      const int bh = o >> 7, d = o & 127;
      float M = -INFINITY;
#pragma unroll
      for (int s = 0; s < NSA; ++s) M = fmaxf(M, pml[(bh * NSA + s) * 2]);
      float L = 0.f, O = 0.f;
#pragma unroll
      for (int s = 0; s < NSA; ++s) {
        const float f = exp2f(pml[(bh * NSA + s) * 2] - M);
        L += f * pml[(bh * NSA + s) * 2 + 1];
        O += f * po[(bh * NSA + s) * 128 + d];
      }
      vec[o] = bf2f(f2bf(O / L));
    }
    return;
  }
  const int h = blockIdx.x >> 3, b = (blockIdx.x >> 1) & 3, dh = blockIdx.x & 1;
  const int tc = t & 7, tr = t >> 3;
  u32x4 wv[16];
#pragma unroll
  for (int i = 0; i < 16; ++i) wv[i] = ldnt<u32x4>(wuv + ((size_t)(tr + 32 * i) * 32 + h) * 128 + dh * 64 + tc * 8);
  const float* pml = (const float*)(ws + WS_PBML);
  if (t < 64) {
    float mm = (t < 32) ? pml[((b * NSB + t) * 32 + h) * 2] : -INFINITY;
    float ll = (t < 32) ? pml[((b * NSB + t) * 32 + h) * 2 + 1] : 0.f;
    float M = mm;
#pragma unroll
    for (int o = 1; o < 64; o <<= 1) M = fmaxf(M, __shfl_xor(M, o));
    float f = exp2f(mm - M);
    float L = f * ll;
#pragma unroll
    for (int o = 1; o < 64; o <<= 1) L += __shfl_xor(L, o);
    if (t < 32) sw[t] = f / L;
  }
  __syncthreads();
  {
    const float* pc = (const float*)(ws + WS_PBC) + ((size_t)b * NSB * 32 + h) * 512 + 2 * t;
    float a0 = 0.f, a1 = 0.f;
#pragma unroll
    for (int s = 0; s < NSB; ++s) {
      const float2 v = *(const float2*)(pc + (size_t)s * 32 * 512);
      a0 += sw[s] * v.x; a1 += sw[s] * v.y;
    }
    sc[2 * t] = a0; sc[2 * t + 1] = a1;
  }
  __syncthreads();
  float acc[8];
#pragma unroll
  for (int e = 0; e < 8; ++e) acc[e] = 0.f;
#pragma unroll
  for (int i = 0; i < 16; ++i) {
    const float cv = sc[tr + 32 * i];
#pragma unroll
    for (int e = 0; e < 4; ++e) { acc[2 * e] += cv * lo2f(wv[i][e]); acc[2 * e + 1] += cv * hi2f(wv[i][e]); }
  }
#pragma unroll
  for (int e = 0; e < 8; ++e) red[tr * 64 + tc * 8 + e] = acc[e];
  __syncthreads();
  if (t < 64) {
    float s = 0.f;
    for (int i = 0; i < 32; ++i) s += red[i * 64 + t];
    vec[16384 + b * 4096 + h * 128 + dh * 64 + t] = bf2f(f2bf(s));
  }
}

__global__ __launch_bounds__(256) void k4_out(const u16* __restrict__ woA, const u16* __restrict__ woB,
                                              char* __restrict__ ws) {
  __shared__ float red[4][4][512];
  const int t = threadIdx.x, part = blockIdx.x >> 7, blk = blockIdx.x & 127;
  const u16* wo = part ? woB : woA;
  const float* vec = (const float*)(ws + WS_VEC) + part * 16384;
  const int tc = t & 63, tr = t >> 6;
  u32x4 wv[8];
#pragma unroll
  for (int i = 0; i < 8; ++i) wv[i] = ldnt<u32x4>(wo + (size_t)(blk * 32 + tr + 4 * i) * 512 + tc * 8);
  float acc[4][8];
#pragma unroll
  for (int b = 0; b < 4; ++b)
#pragma unroll
    for (int e = 0; e < 8; ++e) acc[b][e] = 0.f;
#pragma unroll
  for (int i = 0; i < 8; ++i) {
    float xv[4];
#pragma unroll
    for (int b = 0; b < 4; ++b) xv[b] = vec[b * 4096 + blk * 32 + tr + 4 * i];
#pragma unroll
    for (int e = 0; e < 4; ++e) {
      const float w0 = lo2f(wv[i][e]), w1 = hi2f(wv[i][e]);
#pragma unroll
      for (int b = 0; b < 4; ++b) { acc[b][2 * e] += xv[b] * w0; acc[b][2 * e + 1] += xv[b] * w1; }
    }
  }
#pragma unroll
  for (int b = 0; b < 4; ++b)
#pragma unroll
    for (int e = 0; e < 8; ++e) red[tr][b][tc * 8 + e] = acc[b][e];
  __syncthreads();
  float* slab = (float*)(ws + WS_SLAB) + (size_t)(part * 128 + blk) * 2048;
  for (int o = t; o < 2048; o += 256) slab[o] = (red[0][o >> 9][o & 511] + red[1][o >> 9][o & 511]) + (red[2][o >> 9][o & 511] + red[3][o >> 9][o & 511]);
}

__global__ __launch_bounds__(256) void k5_final(const char* __restrict__ ws, u16* __restrict__ out) {
  const int o = blockIdx.x * 256 + threadIdx.x;
  const int part = o >> 11, r = o & 2047;
  const float* slab = (const float*)(ws + WS_SLAB) + (size_t)part * 128 * 2048 + r;
  float s0 = 0.f, s1 = 0.f;
#pragma unroll 8
  for (int i = 0; i < 128; i += 2) { s0 += slab[(size_t)i * 2048]; s1 += slab[(size_t)(i + 1) * 2048]; }
  const int b = r >> 9, m = r & 511;
  out[b * 1024 + part * 512 + m] = f2bf(s0 + s1);
}

extern "C" void kernel_launch(void* const* d_in, const int* in_sizes, int n_in,
                              void* d_out, int out_size, void* d_ws, size_t ws_size,
                              hipStream_t stream) {
  const u16* xA  = (const u16*)d_in[0];
  const u16* kc  = (const u16*)d_in[1];
  const u16* vc  = (const u16*)d_in[2];
  const u16* wqA = (const u16*)d_in[3];
  const u16* woA = (const u16*)d_in[4];
  const u16* xB  = (const u16*)d_in[5];
  const u16* ckv = (const u16*)d_in[6];
  const u16* wqB = (const u16*)d_in[7];
  const u16* wuk = (const u16*)d_in[8];
  const u16* wuv = (const u16*)d_in[9];
  const u16* woB = (const u16*)d_in[10];
  char* ws = (char*)d_ws;
  hipFuncSetAttribute((const void*)k2_attn, hipFuncAttributeMaxDynamicSharedMemorySize, K2_LDS);
  k1_proj<<<256, 256, 0, stream>>>(xA, wqA, xB, wqB, ws);
  k_abs<<<256, 256, 0, stream>>>(wuk, ws);
  k2_attn<<<256, 256, K2_LDS, stream>>>(kc, vc, ckv, ws);
  k3_combine<<<272, 256, 0, stream>>>(wuv, ws);
  k4_out<<<256, 256, 0, stream>>>(woA, woB, ws);
  k5_final<<<16, 256, 0, stream>>>(ws, (u16*)d_out);
}
```

```cpp
#include <hip/hip_runtime.h>
#include <hip/hip_bf16.h>
#include <stdint.h>

constexpr int NB = 4;
constexpr int SK = 8192;
constexpr int WIN = 2048;
constexpr int NSA = 4;
constexpr int NSB = 32;
constexpr float LOG2E = 1.4426950408889634f;
constexpr float SCALE = 0.08838834764831845f;

constexpr size_t WS_QA   = 0;
constexpr size_t WS_QABS = 65536;
constexpr size_t WS_PAML = WS_QABS + 131072;
constexpr size_t WS_PAO  = WS_PAML + 65536;
constexpr size_t WS_PBML = WS_PAO + 262144 * 2;
constexpr size_t WS_VEC  = WS_PBML + 65536;
constexpr size_t WS_QB   = WS_VEC + 262144;
constexpr size_t WS_SLAB = WS_QB + 65536;
constexpr size_t WS_PBC  = WS_SLAB + 2097152;

typedef __attribute__((ext_vector_type(8))) short bf16x8;
typedef __attribute__((ext_vector_type(4))) short s16x4;
typedef __attribute__((ext_vector_type(16))) float f32x16;
typedef __attribute__((ext_vector_type(4))) float f32x4;
typedef __attribute__((ext_vector_type(4))) unsigned u32x4;
typedef __attribute__((ext_vector_type(2))) __bf16 bf16x2v;
typedef unsigned short u16;

__device__ __forceinline__ float bf2f(u16 v) { return __uint_as_float(((unsigned)v) << 16); }
__device__ __forceinline__ float lo2f(unsigned v) { return __uint_as_float(v << 16); }
__device__ __forceinline__ float hi2f(unsigned v) { return __uint_as_float(v & 0xffff0000u); }
__device__ __forceinline__ unsigned cvtpk(float lo, float hi) {
  unsigned r; asm volatile("v_cvt_pk_bf16_f32 %0, %1, %2" : "=v"(r) : "v"(lo), "v"(hi)); return r;
}
__device__ __forceinline__ u16 f2bf(float x) { return (u16)(cvtpk(x, 0.f) & 0xffffu); }
template <typename T> __device__ __forceinline__ T ldnt(const void* p) { return __builtin_nontemporal_load((const T*)p); }
__device__ __forceinline__ float dot2(unsigned k, unsigned q, float acc) {
  return __builtin_amdgcn_fdot2_f32_bf16(__builtin_bit_cast(bf16x2v, k), __builtin_bit_cast(bf16x2v, q), acc, false);
}

__global__ __launch_bounds__(256) void k1_proj(const u16* __restrict__ xA, const u16* __restrict__ wqA,
                                               const u16* __restrict__ xB, const u16* __restrict__ wqB,
                                               char* __restrict__ ws) {
  __shared__ float sx[512 * 4];
  __shared__ float red[64 * 128];
  const int t = threadIdx.x;
  const bool isA = blockIdx.x < 128;
  const u16* x = isA ? xA : xB;
  const u16* wq = isA ? wqA : wqB;
  u16* dst = (u16*)(ws + (isA ? WS_QA : WS_QB));
  const int col0 = (blockIdx.x & 127) * 32;
  const int tc = t & 3, tr = t >> 2;
  u32x4 wv[8];
#pragma unroll
  for (int i = 0; i < 8; ++i) wv[i] = ldnt<u32x4>(wq + (size_t)(tr + 64 * i) * 4096 + col0 + tc * 8);
  for (int i = t; i < 2048; i += 256) { int b = i >> 9, r = i & 511; sx[r * 4 + b] = bf2f(x[i]); }
  __syncthreads();
  float acc[4][8];
#pragma unroll
  for (int b = 0; b < 4; ++b)
#pragma unroll
    for (int e = 0; e < 8; ++e) acc[b][e] = 0.f;
#pragma unroll
  for (int i = 0; i < 8; ++i) {
    const f32x4 xv = *(const f32x4*)(sx + (tr + 64 * i) * 4);
#pragma unroll
    for (int e = 0; e < 4; ++e) {
      float w0 = lo2f(wv[i][e]), w1 = hi2f(wv[i][e]);
#pragma unroll
      for (int b = 0; b < 4; ++b) { acc[b][2 * e] += xv[b] * w0; acc[b][2 * e + 1] += xv[b] * w1; }
    }
  }
#pragma unroll
  for (int b = 0; b < 4; ++b)
#pragma unroll
    for (int e = 0; e < 8; ++e) red[tr * 128 + b * 32 + tc * 8 + e] = acc[b][e];
  __syncthreads();
  if (t < 128) {
    float s = 0.f;
    for (int i = 0; i < 64; ++i) s += red[i * 128 + t];
    int b = t >> 5, c = t & 31;
    dst[b * 4096 + col0 + c] = f2bf(s);
  }
}

__global__ __launch_bounds__(256) void k_abs(const u16* __restrict__ wuk, char* __restrict__ ws) {
  const int t = threadIdx.x, h = blockIdx.x >> 3, c0 = (blockIdx.x & 7) * 64;
  const int tc = t & 15, tr = t >> 4;
  u32x4 uk[4];
#pragma unroll
  for (int i = 0; i < 4; ++i) uk[i] = ldnt<u32x4>(wuk + ((size_t)(c0 + tr + 16 * i) * 32 + h) * 128 + tc * 8);
  const u16* qb = (const u16*)(ws + WS_QB);
  float qv[4][8];
#pragma unroll
  for (int b = 0; b < 4; ++b) {
    u32x4 q = *(const u32x4*)(qb + b * 4096 + h * 128 + tc * 8);
#pragma unroll
    for (int e = 0; e < 4; ++e) { qv[b][2 * e] = lo2f(q[e]); qv[b][2 * e + 1] = hi2f(q[e]); }
  }
#pragma unroll
  for (int i = 0; i < 4; ++i) {
    float s[4] = {0.f, 0.f, 0.f, 0.f};
#pragma unroll
    for (int e = 0; e < 4; ++e) {
      float w0 = lo2f(uk[i][e]), w1 = hi2f(uk[i][e]);
#pragma unroll
      for (int b = 0; b < 4; ++b) s[b] += qv[b][2 * e] * w0 + qv[b][2 * e + 1] * w1;
    }
#pragma unroll
    for (int b = 0; b < 4; ++b) {
      s[b] += __shfl_xor(s[b], 1); s[b] += __shfl_xor(s[b], 2); s[b] += __shfl_xor(s[b], 4); s[b] += __shfl_xor(s[b], 8);
    }
    if (tc < 4) {
      float v = (tc == 0) ? s[0] : (tc == 1) ? s[1] : (tc == 2) ? s[2] : s[3];
      int c = c0 + tr + 16 * i;
      ((u16*)(ws + WS_QABS))[(tc * 32 + h) * 512 + c] = f2bf(v * (SCALE * LOG2E));
    }
  }
}

constexpr int K2_LDS = 163840;
__device__ __forceinline__ unsigned off_b(unsigned row, unsigned ch) {
  return 256u * row + 16u * (ch ^ (((row & 3) << 2) | ((row >> 2) & 3)));
}

__device__ __forceinline__ void attnB(int wg, const u16* __restrict__ ckv, char* __restrict__ ws, char* smem) {
  const int b = wg >> 5, split = wg & 31;
  const int tid = threadIdx.x, w = tid >> 6, l = tid & 63;
  const int h5 = l >> 5, r = l & 31;
  char* ring = smem + w * 32768;
  float* sred = (float*)(smem + 131072);
  const u16* qabs = (const u16*)(ws + WS_QABS);
  char* qlds = smem + 131072 + w * 8192;
#pragma unroll
  for (int n = 0; n < 8; ++n) {
    const int row = 4 * n + (l >> 4);
    const int ch = (l & 15) ^ (((l >> 4) << 2) | (n & 3));
    const u16* g = qabs + (b * 32 + row) * 512 + w * 128 + ch * 8;
    __builtin_amdgcn_global_load_lds((const unsigned*)g, (unsigned*)(qlds + n * 1024), 16, 0, 0);
  }
  const u16* src0 = ckv + ((size_t)b * SK + split * 256 + (l >> 4)) * 512 + w * 128;
  auto issue = [&](int t) {
#pragma unroll
    for (int n = 0; n < 8; ++n) {
      const int ch = (l & 15) ^ (((l >> 4) << 2) | (n & 3));
      const u16* g = src0 + (size_t)(t * 32 + 4 * n) * 512 + ch * 8;
      __builtin_amdgcn_global_load_lds((const unsigned*)g, (unsigned*)(ring + (t & 3) * 8192 + n * 1024), 16, 0, 2);
    }
  };
  issue(0); issue(1); issue(2); issue(3);
  asm volatile("s_waitcnt vmcnt(32)" ::: "memory");
  bf16x8 qf[8];
#pragma unroll
  for (int ks = 0; ks < 8; ++ks) qf[ks] = *(const bf16x8*)(qlds + off_b(r, 2 * ks + h5));
  asm volatile("s_waitcnt lgkmcnt(0)" ::: "memory");
  __builtin_amdgcn_s_barrier();
  f32x16 acc[4];
#pragma unroll
  for (int cb = 0; cb < 4; ++cb)
#pragma unroll
    for (int i = 0; i < 16; ++i) acc[cb][i] = 0.f;
  float mrun = -INFINITY, lrun = 0.f;
  unsigned rowoff[8];
#pragma unroll
  for (int ks = 0; ks < 8; ++ks) rowoff[ks] = off_b(r, 2 * ks + h5);
  const unsigned q4 = (l & 15) >> 2, p4 = l & 3, blk = (l >> 4) & 1;
#pragma unroll
  for (int it = 0; it < 4; ++it) {
    if (it < 3) asm volatile("s_waitcnt vmcnt(16)" ::: "memory");
    else asm volatile("s_waitcnt vmcnt(0)" ::: "memory");
    char* tile0 = ring + ((2 * it) & 3) * 8192;
    char* tile1 = ring + ((2 * it + 1) & 3) * 8192;
    f32x16 sa, sb16;
#pragma unroll
    for (int i = 0; i < 16; ++i) { sa[i] = 0.f; sb16[i] = 0.f; }
#pragma unroll
    for (int ks = 0; ks < 8; ++ks) {
      bf16x8 a0 = *(const bf16x8*)(tile0 + rowoff[ks]);
      bf16x8 a1 = *(const bf16x8*)(tile1 + rowoff[ks]);
      sa = __builtin_amdgcn_mfma_f32_32x32x16_bf16(a0, qf[ks], sa, 0, 0, 0);
      sb16 = __builtin_amdgcn_mfma_f32_32x32x16_bf16(a1, qf[ks], sb16, 0, 0, 0);
    }
    float* sbuf = sred;
#pragma unroll
    for (int g = 0; g < 4; ++g) {
      f32x4 v = {sa[4 * g], sa[4 * g + 1], sa[4 * g + 2], sa[4 * g + 3]};
      *(f32x4*)(sbuf + w * 1024 + (g * 64 + l) * 4) = v;
    }
    f32x4 vb[4];
#pragma unroll
    for (int g = 0; g < 4; ++g) {
      f32x4 v = {sb16[4 * g], sb16[4 * g + 1], sb16[4 * g + 2], sb16[4 * g + 3]};
      *(f32x4*)(sbuf + 4096 + w * 1024 + (g * 64 + l) * 4) = v;
      (void)vb;
    }
    asm volatile("s_waitcnt lgkmcnt(0)" ::: "memory");
    __builtin_amdgcn_s_barrier();
    float s2[32];
#pragma unroll
    for (int tt = 0; tt < 2; ++tt)
#pragma unroll
      for (int g = 0; g < 4; ++g) {
        const float* base2 = sbuf + tt * 4096;
        f32x4 v0 = *(const f32x4*)(base2 + 0 * 1024 + (g * 64 + l) * 4);
        f32x4 v1 = *(const f32x4*)(base2 + 1 * 1024 + (g * 64 + l) * 4);
        f32x4 v2 = *(const f32x4*)(base2 + 2 * 1024 + (g * 64 + l) * 4);
        f32x4 v3 = *(const f32x4*)(base2 + 3 * 1024 + (g * 64 + l) * 4);
        f32x4 v = (v0 + v1) + (v2 + v3);
        s2[16 * tt + 4 * g] = v[0]; s2[16 * tt + 4 * g + 1] = v[1]; s2[16 * tt + 4 * g + 2] = v[2]; s2[16 * tt + 4 * g + 3] = v[3];
      }
    asm volatile("s_waitcnt lgkmcnt(0)" ::: "memory");
    __builtin_amdgcn_s_barrier();
    float mloc = s2[0];
#pragma unroll
    for (int i = 1; i < 32; ++i) mloc = fmaxf(mloc, s2[i]);
    mloc = fmaxf(mloc, __shfl_xor(mloc, 32));
    const float mnew = fmaxf(mrun, mloc);
    const float alpha = exp2f(mrun - mnew);
    mrun = mnew;
    float ps = 0.f;
#pragma unroll
    for (int i = 0; i < 32; ++i) { s2[i] = exp2f(s2[i] - mnew); ps += s2[i]; }
    lrun = lrun * alpha + ps;
    bf16x8 pf[4];
#pragma unroll
    for (int sq = 0; sq < 4; ++sq) {
      u32x4 u;
#pragma unroll
      for (int j = 0; j < 4; ++j) u[j] = cvtpk(s2[8 * sq + 2 * j], s2[8 * sq + 2 * j + 1]);
      pf[sq] = __builtin_bit_cast(bf16x8, u);
    }
#pragma unroll
    for (int cb = 0; cb < 4; ++cb)
#pragma unroll
      for (int i = 0; i < 16; ++i) acc[cb][i] *= alpha;
#pragma unroll
    for (int tt = 0; tt < 2; ++tt) {
      const char* tile = tt ? tile1 : tile0;
#pragma unroll
      for (int cb = 0; cb < 4; ++cb)
#pragma unroll
        for (int sk = 0; sk < 2; ++sk) {
          const unsigned a0 = off_b(16 * sk + 4 * h5 + q4, 4 * cb + 2 * blk + (p4 >> 1)) + 8 * (p4 & 1);
          const unsigned a1 = off_b(16 * sk + 8 + 4 * h5 + q4, 4 * cb + 2 * blk + (p4 >> 1)) + 8 * (p4 & 1);
          s16x4 v0 = __builtin_amdgcn_ds_read_tr16_b64_v4i16((__attribute__((address_space(3))) s16x4*)(tile + a0));
          s16x4 v1 = __builtin_amdgcn_ds_read_tr16_b64_v4i16((__attribute__((address_space(3))) s16x4*)(tile + a1));
          bf16x8 a = {v0[0], v0[1], v0[2], v0[3], v1[0], v1[1], v1[2], v1[3]};
          acc[cb] = __builtin_amdgcn_mfma_f32_32x32x16_bf16(a, pf[2 * tt + sk], acc[cb], 0, 0, 0);
        }
    }
    if (it < 2) { asm volatile("s_waitcnt lgkmcnt(0)" ::: "memory"); issue(2 * it + 4); issue(2 * it + 5); }
  }
  lrun += __shfl_xor(lrun, 32);
  if (w == 0 && l < 32) {
    float* pml = (float*)(ws + WS_PBML) + ((size_t)(b * NSB + split) * 32 + r) * 2;
    pml[0] = mrun; pml[1] = lrun;
  }
  __syncthreads();
  float* tr = (float*)smem;
#pragma unroll
  for (int cb = 0; cb < 4; ++cb)
#pragma unroll
    for (int g = 0; g < 4; ++g) {
      f32x4 v = {acc[cb][4 * g], acc[cb][4 * g + 1], acc[cb][4 * g + 2], acc[cb][4 * g + 3]};
      *(f32x4*)(tr + r * 516 + w * 128 + 32 * cb + 8 * g + 4 * h5) = v;
    }
  __syncthreads();
  float* pc = (float*)(ws + WS_PBC) + (size_t)(b * NSB + split) * 32 * 512;
#pragma unroll
  for (int i = 0; i < 16; ++i) {
    const int idx = (i * 256 + tid) * 4;
    const int hh = idx >> 9, c = idx & 511;
    *(f32x4*)(pc + idx) = *(const f32x4*)(tr + hh * 516 + c);
  }
}

__device__ __forceinline__ void attnA(int wg, const u16* __restrict__ kc, const u16* __restrict__ vc, char* __restrict__ ws, char* smem) {
  const int b = wg >> 5, hkv = (wg >> 2) & 7, split = wg & 3;
  const int tid = threadIdx.x, w = tid >> 6, l = tid & 63;
  const int r4 = l >> 4, cc = l & 15;
  const size_t base = ((size_t)(b * 8 + hkv) * SK + (SK - WIN) + split * 512 + w * 128) * 128;
  const u16* kp = kc + base + (size_t)r4 * 128 + cc * 8;
  const u16* vp = vc + base + (size_t)r4 * 128 + cc * 8;
  const u16* qa = (const u16*)(ws + WS_QA);
  u32x4 qd[4];
#pragma unroll
  for (int g = 0; g < 4; ++g) qd[g] = *(const u32x4*)(qa + b * 4096 + (hkv * 4 + g) * 128 + cc * 8);
  u32x4 kf[8][4];
#pragma unroll
  for (int ch = 0; ch < 8; ++ch)
#pragma unroll
    for (int i = 0; i < 4; ++i) kf[ch][i] = __builtin_nontemporal_load((const u32x4*)(kp + (size_t)(ch * 16 + 4 * i) * 128));
  float sc[8];
#pragma unroll
  for (int ch = 0; ch < 8; ++ch) {
    float v[16];
#pragma unroll
    for (int i = 0; i < 4; ++i)
#pragma unroll
      for (int g = 0; g < 4; ++g) {
        float a = 0.f;
#pragma unroll
        for (int e = 0; e < 4; ++e) a = dot2(kf[ch][i][e], qd[g][e], a);
        v[i * 4 + g] = a;
      }
    float v8[8], v4[4], v2[2];
#pragma unroll
    for (int k = 0; k < 8; ++k) {
      float keep = (cc & 8) ? v[k + 8] : v[k], send = (cc & 8) ? v[k] : v[k + 8];
      v8[k] = keep + __shfl_xor(send, 8);
    }
#pragma unroll
    for (int k = 0; k < 4; ++k) {
      float keep = (cc & 4) ? v8[k + 4] : v8[k], send = (cc & 4) ? v8[k] : v8[k + 4];
      v4[k] = keep + __shfl_xor(send, 4);
    }
#pragma unroll
    for (int k = 0; k < 2; ++k) {
      float keep = (cc & 2) ? v4[k + 2] : v4[k], send = (cc & 2) ? v4[k] : v4[k + 2];
      v2[k] = keep + __shfl_xor(send, 2);
    }
    {
      float keep = (cc & 1) ? v2[1] : v2[0], send = (cc & 1) ? v2[0] : v2[1];
      sc[ch] = (keep + __shfl_xor(send, 1)) * (SCALE * LOG2E);
    }
  }
  u32x4 vf[8][4];
#pragma unroll
  for (int ch = 0; ch < 8; ++ch)
#pragma unroll
    for (int i = 0; i < 4; ++i) vf[ch][i] = __builtin_nontemporal_load((const u32x4*)(vp + (size_t)(ch * 16 + 4 * i) * 128));
  float m = sc[0];
#pragma unroll
  for (int ch = 1; ch < 8; ++ch) m = fmaxf(m, sc[ch]);
  m = fmaxf(m, __shfl_xor(m, 4)); m = fmaxf(m, __shfl_xor(m, 8)); m = fmaxf(m, __shfl_xor(m, 16)); m = fmaxf(m, __shfl_xor(m, 32));
  float ls = 0.f;
#pragma unroll
  for (int ch = 0; ch < 8; ++ch) { sc[ch] = exp2f(sc[ch] - m); ls += sc[ch]; }
  ls += __shfl_xor(ls, 4); ls += __shfl_xor(ls, 8); ls += __shfl_xor(ls, 16); ls += __shfl_xor(ls, 32);
  float acc[4][8];
#pragma unroll
  for (int g = 0; g < 4; ++g)
#pragma unroll
    for (int e = 0; e < 8; ++e) acc[g][e] = 0.f;
#pragma unroll
  for (int ch = 0; ch < 8; ++ch) {
#pragma unroll
    for (int i = 0; i < 4; ++i) {
      float vv[8];
#pragma unroll
      for (int e = 0; e < 4; ++e) { vv[2 * e] = lo2f(vf[ch][i][e]); vv[2 * e + 1] = hi2f(vf[ch][i][e]); }
#pragma unroll
      for (int g = 0; g < 4; ++g) {
        const float p = __shfl(sc[ch], (l & 48) + 4 * i + g);
#pragma unroll
        for (int e = 0; e < 8; ++e) acc[g][e] += p * vv[e];
      }
    }
  }
#pragma unroll
  for (int g = 0; g < 4; ++g)
#pragma unroll
    for (int e = 0; e < 8; ++e) { acc[g][e] += __shfl_xor(acc[g][e], 16); acc[g][e] += __shfl_xor(acc[g][e], 32); }
  float* sm = (float*)smem;
  float* so = sm + 64;
  if (l < 4) { sm[(w * 4 + l) * 2] = m; sm[(w * 4 + l) * 2 + 1] = ls; }
  if (r4 == 0) {
#pragma unroll
    for (int g = 0; g < 4; ++g)
#pragma unroll
      for (int e = 0; e < 8; ++e) so[(w * 4 + g) * 128 + cc * 8 + e] = acc[g][e];
  }
  __syncthreads();
  for (int o = tid; o < 512; o += 256) {
    const int g = o >> 7, d = o & 127;
    float M = -INFINITY;
#pragma unroll
    for (int ww = 0; ww < 4; ++ww) M = fmaxf(M, sm[(ww * 4 + g) * 2]);
    float L = 0.f, O = 0.f;
#pragma unroll
    for (int ww = 0; ww < 4; ++ww) {
      const float f = exp2f(sm[(ww * 4 + g) * 2] - M);
      L += f * sm[(ww * 4 + g) * 2 + 1];
      O += f * so[(ww * 4 + g) * 128 + d];
    }
    const int hq = hkv * 4 + g;
    ((float*)(ws + WS_PAO))[((size_t)(b * 32 + hq) * NSA + split) * 128 + d] = O;
    if (d == 0) {
      float* pml = (float*)(ws + WS_PAML) + ((size_t)(b * 32 + hq) * NSA + split) * 2;
      pml[0] = M; pml[1] = L;
    }
  }
}

__global__ __launch_bounds__(256, 1) void k2_attn(const u16* __restrict__ kc, const u16* __restrict__ vc,
                                                  const u16* __restrict__ ckv, char* __restrict__ ws) {
  extern __shared__ __attribute__((aligned(16))) char smem[];
  if (blockIdx.x < 128) attnB(blockIdx.x, ckv, ws, smem);
  else attnA(blockIdx.x - 128, kc, vc, ws, smem);
}

__global__ __launch_bounds__(256) void k3_combine(const u16* __restrict__ wuv, char* __restrict__ ws) {
  __shared__ float sw[32];
  __shared__ float sc[512];
  __shared__ float red[32 * 64];
  const int t = threadIdx.x;
  float* vec = (float*)(ws + WS_VEC);
  if (blockIdx.x >= 256) {
    const float* pml = (const float*)(ws + WS_PAML);
    const float* po = (const float*)(ws + WS_PAO);
    for (int o = (blockIdx.x - 256) * 256 + t; o < 16384; o += 4096) {
      const int bh = o >> 7, d = o & 127;
      float M = -INFINITY;
#pragma unroll
      for (int s = 0; s < NSA; ++s) M = fmaxf(M, pml[(bh * NSA + s) * 2]);
      float L = 0.f, O = 0.f;
#pragma unroll
      for (int s = 0; s < NSA; ++s) {
        const float f = exp2f(pml[(bh * NSA + s) * 2] - M);
        L += f * pml[(bh * NSA + s) * 2 + 1];
        O += f * po[(bh * NSA + s) * 128 + d];
      }
      vec[o] = bf2f(f2bf(O / L));
    }
    return;
  }
  const int h = blockIdx.x >> 3, b = (blockIdx.x >> 1) & 3, dh = blockIdx.x & 1;
  const int tc = t & 7, tr = t >> 3;
  u32x4 wv[16];
#pragma unroll
  for (int i = 0; i < 16; ++i) wv[i] = ldnt<u32x4>(wuv + ((size_t)(tr + 32 * i) * 32 + h) * 128 + dh * 64 + tc * 8);
  const float* pml = (const float*)(ws + WS_PBML);
  if (t < 64) {
    float mm = (t < 32) ? pml[((b * NSB + t) * 32 + h) * 2] : -INFINITY;
    float ll = (t < 32) ? pml[((b * NSB + t) * 32 + h) * 2 + 1] : 0.f;
    float M = mm;
#pragma unroll
    for (int o = 1; o < 64; o <<= 1) M = fmaxf(M, __shfl_xor(M, o));
    float f = exp2f(mm - M);
    float L = f * ll;
#pragma unroll
    for (int o = 1; o < 64; o <<= 1) L += __shfl_xor(L, o);
    if (t < 32) sw[t] = f / L;
  }
  __syncthreads();
  {
    const float* pc = (const float*)(ws + WS_PBC) + ((size_t)b * NSB * 32 + h) * 512 + 2 * t;
    float a0 = 0.f, a1 = 0.f;
#pragma unroll
    for (int s = 0; s < NSB; ++s) {
      const float2 v = *(const float2*)(pc + (size_t)s * 32 * 512);
      a0 += sw[s] * v.x; a1 += sw[s] * v.y;
    }
    sc[2 * t] = a0; sc[2 * t + 1] = a1;
  }
  __syncthreads();
  float acc[8];
#pragma unroll
  for (int e = 0; e < 8; ++e) acc[e] = 0.f;
#pragma unroll
  for (int i = 0; i < 16; ++i) {
    const float cv = sc[tr + 32 * i];
#pragma unroll
    for (int e = 0; e < 4; ++e) { acc[2 * e] += cv * lo2f(wv[i][e]); acc[2 * e + 1] += cv * hi2f(wv[i][e]); }
  }
#pragma unroll
  for (int e = 0; e < 8; ++e) red[tr * 64 + tc * 8 + e] = acc[e];
  __syncthreads();
  if (t < 64) {
    float s = 0.f;
    for (int i = 0; i < 32; ++i) s += red[i * 64 + t];
    vec[16384 + b * 4096 + h * 128 + dh * 64 + t] = bf2f(f2bf(s));
  }
}

__global__ __launch_bounds__(256) void k4_out(const u16* __restrict__ woA, const u16* __restrict__ woB,
                                              char* __restrict__ ws) {
  __shared__ float red[4][4][512];
  const int t = threadIdx.x, part = blockIdx.x >> 7, blk = blockIdx.x & 127;
  const u16* wo = part ? woB : woA;
  const float* vec = (const float*)(ws + WS_VEC) + part * 16384;
  const int tc = t & 63, tr = t >> 6;
  u32x4 wv[8];
#pragma unroll
  for (int i = 0; i < 8; ++i) wv[i] = ldnt<u32x4>(wo + (size_t)(blk * 32 + tr + 4 * i) * 512 + tc * 8);
  float acc[4][8];
#pragma unroll
  for (int b = 0; b < 4; ++b)
#pragma unroll
    for (int e = 0; e < 8; ++e) acc[b][e] = 0.f;
#pragma unroll
  for (int i = 0; i < 8; ++i) {
    float xv[4];
#pragma unroll
    for (int b = 0; b < 4; ++b) xv[b] = vec[b * 4096 + blk * 32 + tr + 4 * i];
#pragma unroll
    for (int e = 0; e < 4; ++e) {
      const float w0 = lo2f(wv[i][e]), w1 = hi2f(wv[i][e]);
#pragma unroll
      for (int b = 0; b < 4; ++b) { acc[b][2 * e] += xv[b] * w0; acc[b][2 * e + 1] += xv[b] * w1; }
    }
  }
#pragma unroll
  for (int b = 0; b < 4; ++b)
#pragma unroll
    for (int e = 0; e < 8; ++e) red[tr][b][tc * 8 + e] = acc[b][e];
  __syncthreads();
  float* slab = (float*)(ws + WS_SLAB) + (size_t)(part * 128 + blk) * 2048;
  for (int o = t; o < 2048; o += 256) slab[o] = (red[0][o >> 9][o & 511] + red[1][o >> 9][o & 511]) + (red[2][o >> 9][o & 511] + red[3][o >> 9][o & 511]);
}

__global__ __launch_bounds__(256) void k5_final(const char* __restrict__ ws, u16* __restrict__ out) {
  __shared__ float red[8][32];
  const int t = threadIdx.x, g = t >> 5;
  const int o = blockIdx.x * 32 + (t & 31);
  const int part = o >> 11, r = o & 2047;
  const float* slab = (const float*)(ws + WS_SLAB) + ((size_t)part * 128 + g * 16) * 2048 + r;
  float v[16];
#pragma unroll
  for (int i = 0; i < 16; ++i) v[i] = slab[(size_t)i * 2048];
  float s = 0.f;
#pragma unroll
  for (int i = 0; i < 16; ++i) s += v[i];
  red[g][t & 31] = s;
  __syncthreads();
  if (t < 32) {
    float z = 0.f;
#pragma unroll
    for (int i = 0; i < 8; ++i) z += red[i][t];
    const int b = r >> 9, m = r & 511;
    out[b * 1024 + part * 512 + m] = f2bf(z);
  }
}

extern "C" void kernel_launch(void* const* d_in, const int* in_sizes, int n_in,
                              void* d_out, int out_size, void* d_ws, size_t ws_size,
                              hipStream_t stream) {
  const u16* xA  = (const u16*)d_in[0];
  const u16* kc  = (const u16*)d_in[1];
  const u16* vc  = (const u16*)d_in[2];
  const u16* wqA = (const u16*)d_in[3];
  const u16* woA = (const u16*)d_in[4];
  const u16* xB  = (const u16*)d_in[5];
  const u16* ckv = (const u16*)d_in[6];
  const u16* wqB = (const u16*)d_in[7];
  const u16* wuk = (const u16*)d_in[8];
  const u16* wuv = (const u16*)d_in[9];
  const u16* woB = (const u16*)d_in[10];
  char* ws = (char*)d_ws;
  hipFuncSetAttribute((const void*)k2_attn, hipFuncAttributeMaxDynamicSharedMemorySize, K2_LDS);
  k1_proj<<<256, 256, 0, stream>>>(xA, wqA, xB, wqB, ws);
  k_abs<<<256, 256, 0, stream>>>(wuk, ws);
  k2_attn<<<256, 256, K2_LDS, stream>>>(kc, vc, ckv, ws);
  k3_combine<<<272, 256, 0, stream>>>(wuv, ws);
  k4_out<<<256, 256, 0, stream>>>(woA, woB, ws);
  k5_final<<<128, 256, 0, stream>>>(ws, (u16*)d_out);
}
```

```cpp
#include <hip/hip_runtime.h>
#include <hip/hip_bf16.h>
#include <stdint.h>

constexpr int NB = 4;
constexpr int SK = 8192;
constexpr int WIN = 2048;
constexpr int NSA = 4;
constexpr int NSB = 32;
constexpr float LOG2E = 1.4426950408889634f;
constexpr float SCALE = 0.08838834764831845f;

constexpr size_t WS_QA   = 0;
constexpr size_t WS_QABS = 65536;
constexpr size_t WS_PAML = WS_QABS + 131072;
constexpr size_t WS_PAO  = WS_PAML + 65536;
constexpr size_t WS_PBML = WS_PAO + 262144 * 2;
constexpr size_t WS_VEC  = WS_PBML + 65536;
constexpr size_t WS_QB   = WS_VEC + 262144;
constexpr size_t WS_SLAB = WS_QB + 65536;
constexpr size_t WS_CNT  = WS_VEC;
constexpr size_t WS_PBC  = WS_SLAB + 2097152;

typedef __attribute__((ext_vector_type(8))) short bf16x8;
typedef __attribute__((ext_vector_type(4))) short s16x4;
typedef __attribute__((ext_vector_type(16))) float f32x16;
typedef __attribute__((ext_vector_type(4))) float f32x4;
typedef __attribute__((ext_vector_type(4))) unsigned u32x4;
typedef __attribute__((ext_vector_type(2))) __bf16 bf16x2v;
typedef unsigned short u16;

__device__ __forceinline__ float bf2f(u16 v) { return __uint_as_float(((unsigned)v) << 16); }
__device__ __forceinline__ float lo2f(unsigned v) { return __uint_as_float(v << 16); }
__device__ __forceinline__ float hi2f(unsigned v) { return __uint_as_float(v & 0xffff0000u); }
__device__ __forceinline__ unsigned cvtpk(float lo, float hi) {
  unsigned r; asm volatile("v_cvt_pk_bf16_f32 %0, %1, %2" : "=v"(r) : "v"(lo), "v"(hi)); return r;
}
__device__ __forceinline__ u16 f2bf(float x) { return (u16)(cvtpk(x, 0.f) & 0xffffu); }
template <typename T> __device__ __forceinline__ T ldnt(const void* p) { return __builtin_nontemporal_load((const T*)p); }
__device__ __forceinline__ float dot2(unsigned k, unsigned q, float acc) {
  return __builtin_amdgcn_fdot2_f32_bf16(__builtin_bit_cast(bf16x2v, k), __builtin_bit_cast(bf16x2v, q), acc, false);
}

__global__ __launch_bounds__(512) void k1_proj(const u16* __restrict__ xA, const u16* __restrict__ wqA,
                                               const u16* __restrict__ xB, const u16* __restrict__ wqB,
                                               const u16* __restrict__ wuk, char* __restrict__ ws) {
  __shared__ float sx[512 * 4];
  __shared__ float red[16384];
  __shared__ float sq[512];
  const int t = threadIdx.x;
  if (blockIdx.x == 0 && t < 64) ((unsigned*)(ws + WS_CNT))[t * 32] = 0u;
  float acc[4][8];
#pragma unroll
  for (int b = 0; b < 4; ++b)
#pragma unroll
    for (int e = 0; e < 8; ++e) acc[b][e] = 0.f;
  if (blockIdx.x < 128) {
    const int col0 = blockIdx.x * 32, tc = t & 3, tr = t >> 2;
    u32x4 wv[4];
#pragma unroll
    for (int i = 0; i < 4; ++i) wv[i] = ldnt<u32x4>(wqA + (size_t)(tr + 128 * i) * 4096 + col0 + tc * 8);
    for (int i = t; i < 2048; i += 512) { int b = i >> 9, r = i & 511; sx[r * 4 + b] = bf2f(xA[i]); }
    __syncthreads();
#pragma unroll
    for (int i = 0; i < 4; ++i) {
      const f32x4 xv = *(const f32x4*)(sx + (tr + 128 * i) * 4);
#pragma unroll
      for (int e = 0; e < 4; ++e) {
        float w0 = lo2f(wv[i][e]), w1 = hi2f(wv[i][e]);
#pragma unroll
        for (int b = 0; b < 4; ++b) { acc[b][2 * e] += xv[b] * w0; acc[b][2 * e + 1] += xv[b] * w1; }
      }
    }
#pragma unroll
    for (int b = 0; b < 4; ++b)
#pragma unroll
      for (int e = 0; e < 8; ++e) red[tr * 128 + b * 32 + tc * 8 + e] = acc[b][e];
    __syncthreads();
    {
      const int o = t & 127, g = t >> 7;
      float s = 0.f;
      for (int i = 0; i < 32; ++i) s += red[(g * 32 + i) * 128 + o];
      sq[g * 128 + o] = s;
    }
    __syncthreads();
    if (t < 128) {
      const float s = (sq[t] + sq[128 + t]) + (sq[256 + t] + sq[384 + t]);
      ((u16*)(ws + WS_QA))[(t >> 5) * 4096 + col0 + (t & 31)] = f2bf(s);
    }
  } else {
    const int id = blockIdx.x - 128, h = (id & 7) * 4 + ((id >> 3) & 3), cq = id >> 5;
    const int tc = t & 15, tr = t >> 4;
    u32x4 wv[16], uk[4];
#pragma unroll
    for (int i = 0; i < 16; ++i) wv[i] = *(const u32x4*)(wqB + (size_t)(tr + 32 * i) * 4096 + h * 128 + tc * 8);
#pragma unroll
    for (int i = 0; i < 4; ++i) uk[i] = ldnt<u32x4>(wuk + ((size_t)(cq * 128 + tr + 32 * i) * 32 + h) * 128 + tc * 8);
    for (int i = t; i < 2048; i += 512) { int b = i >> 9, r = i & 511; sx[r * 4 + b] = bf2f(xB[i]); }
    __syncthreads();
#pragma unroll
    for (int i = 0; i < 16; ++i) {
      const f32x4 xv = *(const f32x4*)(sx + (tr + 32 * i) * 4);
#pragma unroll
      for (int e = 0; e < 4; ++e) {
        float w0 = lo2f(wv[i][e]), w1 = hi2f(wv[i][e]);
#pragma unroll
        for (int b = 0; b < 4; ++b) { acc[b][2 * e] += xv[b] * w0; acc[b][2 * e + 1] += xv[b] * w1; }
      }
    }
#pragma unroll
    for (int b = 0; b < 4; ++b)
#pragma unroll
      for (int e = 0; e < 8; ++e) red[tr * 512 + b * 128 + tc * 8 + e] = acc[b][e];
    __syncthreads();
    {
      float s = 0.f;
      for (int i = 0; i < 32; ++i) s += red[i * 512 + t];
      sq[t] = bf2f(f2bf(s));
    }
    __syncthreads();
    float qv[4][8];
#pragma unroll
    for (int b = 0; b < 4; ++b)
#pragma unroll
      for (int e = 0; e < 8; ++e) qv[b][e] = sq[b * 128 + tc * 8 + e];
#pragma unroll
    for (int i = 0; i < 4; ++i) {
      float s[4] = {0.f, 0.f, 0.f, 0.f};
#pragma unroll
      for (int e = 0; e < 4; ++e) {
        float w0 = lo2f(uk[i][e]), w1 = hi2f(uk[i][e]);
#pragma unroll
        for (int b = 0; b < 4; ++b) s[b] += qv[b][2 * e] * w0 + qv[b][2 * e + 1] * w1;
      }
#pragma unroll
      for (int b = 0; b < 4; ++b) {
        s[b] += __shfl_xor(s[b], 1); s[b] += __shfl_xor(s[b], 2); s[b] += __shfl_xor(s[b], 4); s[b] += __shfl_xor(s[b], 8);
      }
      if (tc < 4) {
        float v = (tc == 0) ? s[0] : (tc == 1) ? s[1] : (tc == 2) ? s[2] : s[3];
        ((u16*)(ws + WS_QABS))[(tc * 32 + h) * 512 + cq * 128 + tr + 32 * i] = f2bf(v * (SCALE * LOG2E));
      }
    }
  }
}

constexpr int K2_LDS = 163840;
__device__ __forceinline__ unsigned off_b(unsigned row, unsigned ch) {
  return 256u * row + 16u * (ch ^ (((row & 3) << 2) | ((row >> 2) & 3)));
}

__device__ __forceinline__ void attnB(int wg, const u16* __restrict__ ckv, char* __restrict__ ws, char* smem) {
  const int b = wg >> 5, split = wg & 31;
  const int tid = threadIdx.x, w = tid >> 6, l = tid & 63;
  const int h5 = l >> 5, r = l & 31;
  char* ring = smem + w * 32768;
  float* sred = (float*)(smem + 131072);
  const u16* qabs = (const u16*)(ws + WS_QABS);
  char* qlds = smem + 131072 + w * 8192;
#pragma unroll
  for (int n = 0; n < 8; ++n) {
    const int row = 4 * n + (l >> 4);
    const int ch = (l & 15) ^ (((l >> 4) << 2) | (n & 3));
    const u16* g = qabs + (b * 32 + row) * 512 + w * 128 + ch * 8;
    __builtin_amdgcn_global_load_lds((const unsigned*)g, (unsigned*)(qlds + n * 1024), 16, 0, 0);
  }
  const u16* src0 = ckv + ((size_t)b * SK + split * 256 + (l >> 4)) * 512 + w * 128;
  auto issue = [&](int t) {
#pragma unroll
    for (int n = 0; n < 8; ++n) {
      const int ch = (l & 15) ^ (((l >> 4) << 2) | (n & 3));
      const u16* g = src0 + (size_t)(t * 32 + 4 * n) * 512 + ch * 8;
      __builtin_amdgcn_global_load_lds((const unsigned*)g, (unsigned*)(ring + (t & 3) * 8192 + n * 1024), 16, 0, 2);
    }
  };
  issue(0); issue(1); issue(2); issue(3);
  asm volatile("s_waitcnt vmcnt(32)" ::: "memory");
  bf16x8 qf[8];
#pragma unroll
  for (int ks = 0; ks < 8; ++ks) qf[ks] = *(const bf16x8*)(qlds + off_b(r, 2 * ks + h5));
  asm volatile("s_waitcnt lgkmcnt(0)" ::: "memory");
  __builtin_amdgcn_s_barrier();
  f32x16 acc[4];
#pragma unroll
  for (int cb = 0; cb < 4; ++cb)
#pragma unroll
    for (int i = 0; i < 16; ++i) acc[cb][i] = 0.f;
  float mrun = -INFINITY, lrun = 0.f;
  unsigned rowoff[8];
#pragma unroll
  for (int ks = 0; ks < 8; ++ks) rowoff[ks] = off_b(r, 2 * ks + h5);
  const unsigned q4 = (l & 15) >> 2, p4 = l & 3, blk = (l >> 4) & 1;
#pragma unroll
  for (int it = 0; it < 4; ++it) {
    if (it < 3) asm volatile("s_waitcnt vmcnt(16)" ::: "memory");
    else asm volatile("s_waitcnt vmcnt(0)" ::: "memory");
    char* tile0 = ring + ((2 * it) & 3) * 8192;
    char* tile1 = ring + ((2 * it + 1) & 3) * 8192;
    f32x16 sa, sb16;
#pragma unroll
    for (int i = 0; i < 16; ++i) { sa[i] = 0.f; sb16[i] = 0.f; }
#pragma unroll
    for (int ks = 0; ks < 8; ++ks) {
      bf16x8 a0 = *(const bf16x8*)(tile0 + rowoff[ks]);
      bf16x8 a1 = *(const bf16x8*)(tile1 + rowoff[ks]);
      sa = __builtin_amdgcn_mfma_f32_32x32x16_bf16(a0, qf[ks], sa, 0, 0, 0);
      sb16 = __builtin_amdgcn_mfma_f32_32x32x16_bf16(a1, qf[ks], sb16, 0, 0, 0);
    }
    float* sbuf = sred;
#pragma unroll
    for (int g = 0; g < 4; ++g) {
      f32x4 v = {sa[4 * g], sa[4 * g + 1], sa[4 * g + 2], sa[4 * g + 3]};
      *(f32x4*)(sbuf + w * 1024 + (g * 64 + l) * 4) = v;
    }
    f32x4 vb[4];
#pragma unroll
    for (int g = 0; g < 4; ++g) {
      f32x4 v = {sb16[4 * g], sb16[4 * g + 1], sb16[4 * g + 2], sb16[4 * g + 3]};
      *(f32x4*)(sbuf + 4096 + w * 1024 + (g * 64 + l) * 4) = v;
      (void)vb;
    }
    asm volatile("s_waitcnt lgkmcnt(0)" ::: "memory");
    __builtin_amdgcn_s_barrier();
    float s2[32];
#pragma unroll
    for (int tt = 0; tt < 2; ++tt)
#pragma unroll
      for (int g = 0; g < 4; ++g) {
        const float* base2 = sbuf + tt * 4096;
        f32x4 v0 = *(const f32x4*)(base2 + 0 * 1024 + (g * 64 + l) * 4);
        f32x4 v1 = *(const f32x4*)(base2 + 1 * 1024 + (g * 64 + l) * 4);
        f32x4 v2 = *(const f32x4*)(base2 + 2 * 1024 + (g * 64 + l) * 4);
        f32x4 v3 = *(const f32x4*)(base2 + 3 * 1024 + (g * 64 + l) * 4);
        f32x4 v = (v0 + v1) + (v2 + v3);
        s2[16 * tt + 4 * g] = v[0]; s2[16 * tt + 4 * g + 1] = v[1]; s2[16 * tt + 4 * g + 2] = v[2]; s2[16 * tt + 4 * g + 3] = v[3];
      }
    asm volatile("s_waitcnt lgkmcnt(0)" ::: "memory");
    __builtin_amdgcn_s_barrier();
    float mloc = s2[0];
#pragma unroll
    for (int i = 1; i < 32; ++i) mloc = fmaxf(mloc, s2[i]);
    mloc = fmaxf(mloc, __shfl_xor(mloc, 32));
    const float mnew = fmaxf(mrun, mloc);
    const float alpha = exp2f(mrun - mnew);
    mrun = mnew;
    float ps = 0.f;
#pragma unroll
    for (int i = 0; i < 32; ++i) { s2[i] = exp2f(s2[i] - mnew); ps += s2[i]; }
    lrun = lrun * alpha + ps;
    bf16x8 pf[4];
#pragma unroll
    for (int sq = 0; sq < 4; ++sq) {
      u32x4 u;
#pragma unroll
      for (int j = 0; j < 4; ++j) u[j] = cvtpk(s2[8 * sq + 2 * j], s2[8 * sq + 2 * j + 1]);
      pf[sq] = __builtin_bit_cast(bf16x8, u);
    }
#pragma unroll
    for (int cb = 0; cb < 4; ++cb)
#pragma unroll
      for (int i = 0; i < 16; ++i) acc[cb][i] *= alpha;
#pragma unroll
    for (int tt = 0; tt < 2; ++tt) {
      const char* tile = tt ? tile1 : tile0;
#pragma unroll
      for (int cb = 0; cb < 4; ++cb)
#pragma unroll
        for (int sk = 0; sk < 2; ++sk) {
          const unsigned a0 = off_b(16 * sk + 4 * h5 + q4, 4 * cb + 2 * blk + (p4 >> 1)) + 8 * (p4 & 1);
          const unsigned a1 = off_b(16 * sk + 8 + 4 * h5 + q4, 4 * cb + 2 * blk + (p4 >> 1)) + 8 * (p4 & 1);
          s16x4 v0 = __builtin_amdgcn_ds_read_tr16_b64_v4i16((__attribute__((address_space(3))) s16x4*)(tile + a0));
          s16x4 v1 = __builtin_amdgcn_ds_read_tr16_b64_v4i16((__attribute__((address_space(3))) s16x4*)(tile + a1));
          bf16x8 a = {v0[0], v0[1], v0[2], v0[3], v1[0], v1[1], v1[2], v1[3]};
          acc[cb] = __builtin_amdgcn_mfma_f32_32x32x16_bf16(a, pf[2 * tt + sk], acc[cb], 0, 0, 0);
        }
    }
    if (it < 2) { asm volatile("s_waitcnt lgkmcnt(0)" ::: "memory"); issue(2 * it + 4); issue(2 * it + 5); }
  }
  lrun += __shfl_xor(lrun, 32);
  if (w == 0 && l < 32) {
    float* pml = (float*)(ws + WS_PBML) + ((size_t)(b * NSB + split) * 32 + r) * 2;
    pml[0] = mrun; pml[1] = lrun;
  }
  __syncthreads();
  float* tr = (float*)smem;
#pragma unroll
  for (int cb = 0; cb < 4; ++cb)
#pragma unroll
    for (int g = 0; g < 4; ++g) {
      f32x4 v = {acc[cb][4 * g], acc[cb][4 * g + 1], acc[cb][4 * g + 2], acc[cb][4 * g + 3]};
      *(f32x4*)(tr + r * 516 + w * 128 + 32 * cb + 8 * g + 4 * h5) = v;
    }
  __syncthreads();
  float* pc = (float*)(ws + WS_PBC) + (size_t)(b * NSB + split) * 32 * 512;
#pragma unroll
  for (int i = 0; i < 16; ++i) {
    const int idx = (i * 256 + tid) * 4;
    const int hh = idx >> 9, c = idx & 511;
    *(f32x4*)(pc + idx) = *(const f32x4*)(tr + hh * 516 + c);
  }
}

__device__ __forceinline__ void attnA(int wg, const u16* __restrict__ kc, const u16* __restrict__ vc, char* __restrict__ ws, char* smem) {
  const int b = wg >> 5, hkv = (wg >> 2) & 7, split = wg & 3;
  const int tid = threadIdx.x, w = tid >> 6, l = tid & 63;
  const int r4 = l >> 4, cc = l & 15;
  const size_t base = ((size_t)(b * 8 + hkv) * SK + (SK - WIN) + split * 512 + w * 128) * 128;
  const u16* kp = kc + base + (size_t)r4 * 128 + cc * 8;
  const u16* vp = vc + base + (size_t)r4 * 128 + cc * 8;
  const u16* qa = (const u16*)(ws + WS_QA);
  u32x4 qd[4];
#pragma unroll
  for (int g = 0; g < 4; ++g) qd[g] = *(const u32x4*)(qa + b * 4096 + (hkv * 4 + g) * 128 + cc * 8);
  u32x4 kf[8][4];
#pragma unroll
  for (int ch = 0; ch < 8; ++ch)
#pragma unroll
    for (int i = 0; i < 4; ++i) kf[ch][i] = __builtin_nontemporal_load((const u32x4*)(kp + (size_t)(ch * 16 + 4 * i) * 128));
  u32x4 vf[8][4];
#pragma unroll
  for (int ch = 0; ch < 8; ++ch)
#pragma unroll
    for (int i = 0; i < 4; ++i) vf[ch][i] = __builtin_nontemporal_load((const u32x4*)(vp + (size_t)(ch * 16 + 4 * i) * 128));
  float sc[8];
#pragma unroll
  for (int ch = 0; ch < 8; ++ch) {
    float v[16];
#pragma unroll
    for (int i = 0; i < 4; ++i)
#pragma unroll
      for (int g = 0; g < 4; ++g) {
        float a = 0.f;
#pragma unroll
        for (int e = 0; e < 4; ++e) a = dot2(kf[ch][i][e], qd[g][e], a);
        v[i * 4 + g] = a;
      }
    float v8[8], v4[4], v2[2];
#pragma unroll
    for (int k = 0; k < 8; ++k) {
      float keep = (cc & 8) ? v[k + 8] : v[k], send = (cc & 8) ? v[k] : v[k + 8];
      v8[k] = keep + __shfl_xor(send, 8);
    }
#pragma unroll
    for (int k = 0; k < 4; ++k) {
      float keep = (cc & 4) ? v8[k + 4] : v8[k], send = (cc & 4) ? v8[k] : v8[k + 4];
      v4[k] = keep + __shfl_xor(send, 4);
    }
#pragma unroll
    for (int k = 0; k < 2; ++k) {
      float keep = (cc & 2) ? v4[k + 2] : v4[k], send = (cc & 2) ? v4[k] : v4[k + 2];
      v2[k] = keep + __shfl_xor(send, 2);
    }
    {
      float keep = (cc & 1) ? v2[1] : v2[0], send = (cc & 1) ? v2[0] : v2[1];
      sc[ch] = (keep + __shfl_xor(send, 1)) * (SCALE * LOG2E);
    }
  }
  float m = sc[0];
#pragma unroll
  for (int ch = 1; ch < 8; ++ch) m = fmaxf(m, sc[ch]);
  m = fmaxf(m, __shfl_xor(m, 4)); m = fmaxf(m, __shfl_xor(m, 8)); m = fmaxf(m, __shfl_xor(m, 16)); m = fmaxf(m, __shfl_xor(m, 32));
  float ls = 0.f;
#pragma unroll
  for (int ch = 0; ch < 8; ++ch) { sc[ch] = exp2f(sc[ch] - m); ls += sc[ch]; }
  ls += __shfl_xor(ls, 4); ls += __shfl_xor(ls, 8); ls += __shfl_xor(ls, 16); ls += __shfl_xor(ls, 32);
  float acc[4][8];
#pragma unroll
  for (int g = 0; g < 4; ++g)
#pragma unroll
    for (int e = 0; e < 8; ++e) acc[g][e] = 0.f;
#pragma unroll
  for (int ch = 0; ch < 8; ++ch) {
#pragma unroll
    for (int i = 0; i < 4; ++i) {
      float vv[8];
#pragma unroll
      for (int e = 0; e < 4; ++e) { vv[2 * e] = lo2f(vf[ch][i][e]); vv[2 * e + 1] = hi2f(vf[ch][i][e]); }
#pragma unroll
      for (int g = 0; g < 4; ++g) {
        const float p = __shfl(sc[ch], (l & 48) + 4 * i + g);
#pragma unroll
        for (int e = 0; e < 8; ++e) acc[g][e] += p * vv[e];
      }
    }
  }
#pragma unroll
  for (int g = 0; g < 4; ++g)
#pragma unroll
    for (int e = 0; e < 8; ++e) { acc[g][e] += __shfl_xor(acc[g][e], 16); acc[g][e] += __shfl_xor(acc[g][e], 32); }
  float* sm = (float*)smem;
  float* so = sm + 64;
  if (l < 4) { sm[(w * 4 + l) * 2] = m; sm[(w * 4 + l) * 2 + 1] = ls; }
  if (r4 == 0) {
#pragma unroll
    for (int g = 0; g < 4; ++g)
#pragma unroll
      for (int e = 0; e < 8; ++e) so[(w * 4 + g) * 128 + cc * 8 + e] = acc[g][e];
  }
  __syncthreads();
  for (int o = tid; o < 512; o += 256) {
    const int g = o >> 7, d = o & 127;
    float M = -INFINITY;
#pragma unroll
    for (int ww = 0; ww < 4; ++ww) M = fmaxf(M, sm[(ww * 4 + g) * 2]);
    float L = 0.f, O = 0.f;
#pragma unroll
    for (int ww = 0; ww < 4; ++ww) {
      const float f = exp2f(sm[(ww * 4 + g) * 2] - M);
      L += f * sm[(ww * 4 + g) * 2 + 1];
      O += f * so[(ww * 4 + g) * 128 + d];
    }
    const int hq = hkv * 4 + g;
    ((float*)(ws + WS_PAO))[((size_t)(b * 32 + hq) * NSA + split) * 128 + d] = O;
    if (d == 0) {
      float* pml = (float*)(ws + WS_PAML) + ((size_t)(b * 32 + hq) * NSA + split) * 2;
      pml[0] = M; pml[1] = L;
    }
  }
}

__global__ __launch_bounds__(256, 1) void k2_attn(const u16* __restrict__ kc, const u16* __restrict__ vc,
                                                  const u16* __restrict__ ckv, char* __restrict__ ws) {
  extern __shared__ __attribute__((aligned(16))) char smem[];
  if (blockIdx.x < 128) attnB(blockIdx.x, ckv, ws, smem);
  else attnA(blockIdx.x - 128, kc, vc, ws, smem);
}

__global__ __launch_bounds__(256) void k3_tail(const u16* __restrict__ wuv, const u16* __restrict__ woA,
                                               const u16* __restrict__ woB, char* __restrict__ ws, u16* __restrict__ out) {
  __shared__ float sw[32];
  __shared__ float sc[512];
  __shared__ float red[4 * 512];
  __shared__ float sv[64];
  __shared__ int slast;
  const int t = threadIdx.x;
  const int part = blockIdx.x >> 8, id = blockIdx.x & 255;
  const int h = (id & 7) * 4 + ((id >> 3) & 3), b = (id >> 6) & 3, dh = (id >> 5) & 1;
  const u16* wo = (part ? woB : woA) + (size_t)(h * 128 + dh * 64) * 512;
  const int oc = t & 63, orow = t >> 6;
  u32x4 wov[16];
#pragma unroll
  for (int i = 0; i < 16; ++i) wov[i] = *(const u32x4*)(wo + (size_t)(orow + 4 * i) * 512 + oc * 8);
  if (part == 0) {
    if (t < 64) {
      const float* pml = (const float*)(ws + WS_PAML) + (size_t)(b * 32 + h) * NSA * 2;
      const float* po = (const float*)(ws + WS_PAO) + (size_t)(b * 32 + h) * NSA * 128 + dh * 64 + t;
      float M = -INFINITY;
#pragma unroll
      for (int s = 0; s < NSA; ++s) M = fmaxf(M, pml[s * 2]);
      float L = 0.f, O = 0.f;
#pragma unroll
      for (int s = 0; s < NSA; ++s) { const float f = exp2f(pml[s * 2] - M); L += f * pml[s * 2 + 1]; O += f * po[s * 128]; }
      sv[t] = bf2f(f2bf(O / L));
    }
  } else {
    const int tc = t & 7, tr = t >> 3;
    u32x4 wv[16];
#pragma unroll
    for (int i = 0; i < 16; ++i) wv[i] = ldnt<u32x4>(wuv + ((size_t)(tr + 32 * i) * 32 + h) * 128 + dh * 64 + tc * 8);
    const float* pc = (const float*)(ws + WS_PBC) + ((size_t)b * NSB * 32 + h) * 512 + 2 * t;
    float2 pv[NSB];
#pragma unroll
    for (int s = 0; s < NSB; ++s) pv[s] = *(const float2*)(pc + (size_t)s * 32 * 512);
    const float* pml = (const float*)(ws + WS_PBML);
    if (t < 64) {
      float mm = (t < 32) ? pml[((b * NSB + t) * 32 + h) * 2] : -INFINITY;
      float ll = (t < 32) ? pml[((b * NSB + t) * 32 + h) * 2 + 1] : 0.f;
      float M = mm;
#pragma unroll
      for (int o = 1; o < 64; o <<= 1) M = fmaxf(M, __shfl_xor(M, o));
      float f = exp2f(mm - M);
      float L = f * ll;
#pragma unroll
      for (int o = 1; o < 64; o <<= 1) L += __shfl_xor(L, o);
      if (t < 32) sw[t] = f / L;
    }
    __syncthreads();
    float a0 = 0.f, a1 = 0.f;
#pragma unroll
    for (int s = 0; s < NSB; ++s) { a0 += sw[s] * pv[s].x; a1 += sw[s] * pv[s].y; }
    sc[2 * t] = a0; sc[2 * t + 1] = a1;
    __syncthreads();
    float acc[8];
#pragma unroll
    for (int e = 0; e < 8; ++e) acc[e] = 0.f;
#pragma unroll
    for (int i = 0; i < 16; ++i) {
      const float cv = sc[tr + 32 * i];
#pragma unroll
      for (int e = 0; e < 4; ++e) { acc[2 * e] += cv * lo2f(wv[i][e]); acc[2 * e + 1] += cv * hi2f(wv[i][e]); }
    }
#pragma unroll
    for (int e = 0; e < 8; ++e) red[tr * 64 + tc * 8 + e] = acc[e];
    __syncthreads();
    if (t < 64) {
      float s = 0.f;
      for (int i = 0; i < 32; ++i) s += red[i * 64 + t];
      sv[t] = bf2f(f2bf(s));
    }
  }
  __syncthreads();
  {
    float acc[8];
#pragma unroll
    for (int e = 0; e < 8; ++e) acc[e] = 0.f;
#pragma unroll
    for (int i = 0; i < 16; ++i) {
      const float xv = sv[orow + 4 * i];
#pragma unroll
      for (int e = 0; e < 4; ++e) { acc[2 * e] += xv * lo2f(wov[i][e]); acc[2 * e + 1] += xv * hi2f(wov[i][e]); }
    }
#pragma unroll
    for (int e = 0; e < 8; ++e) red[orow * 512 + oc * 8 + e] = acc[e];
  }
  __syncthreads();
  float* slabs = (float*)(ws + WS_SLAB) + (size_t)(part * 4 + b) * 64 * 512;
  {
    float* sl = slabs + (h * 2 + dh) * 512;
#pragma unroll
    for (int k = 0; k < 2; ++k) {
      const int o = t + 256 * k;
      __hip_atomic_store(sl + o, (red[o] + red[512 + o]) + (red[1024 + o] + red[1536 + o]), __ATOMIC_RELAXED, __HIP_MEMORY_SCOPE_AGENT);
    }
  }
  asm volatile("s_waitcnt vmcnt(0)" ::: "memory");
  __syncthreads();
  if (t == 0) {
    const unsigned old = __hip_atomic_fetch_add((unsigned*)(ws + WS_CNT) + (part * 4 + b) * 32, 1u, __ATOMIC_RELAXED, __HIP_MEMORY_SCOPE_AGENT);
    slast = (old == 63u);
  }
  __syncthreads();
  if (slast) {
#pragma unroll
    for (int k = 0; k < 2; ++k) {
      const int o = t + 256 * k;
      float v[64];
#pragma unroll
      for (int i = 0; i < 64; ++i) v[i] = __hip_atomic_load(slabs + i * 512 + o, __ATOMIC_RELAXED, __HIP_MEMORY_SCOPE_AGENT);
      float s = 0.f;
#pragma unroll
      for (int i = 0; i < 64; ++i) s += v[i];
      out[b * 1024 + part * 512 + o] = f2bf(s);
    }
  }
}

extern "C" void kernel_launch(void* const* d_in, const int* in_sizes, int n_in,
                              void* d_out, int out_size, void* d_ws, size_t ws_size,
                              hipStream_t stream) {
  const u16* xA  = (const u16*)d_in[0];
  const u16* kc  = (const u16*)d_in[1];
  const u16* vc  = (const u16*)d_in[2];
  const u16* wqA = (const u16*)d_in[3];
  const u16* woA = (const u16*)d_in[4];
  const u16* xB  = (const u16*)d_in[5];
  const u16* ckv = (const u16*)d_in[6];
  const u16* wqB = (const u16*)d_in[7];
  const u16* wuk = (const u16*)d_in[8];
  const u16* wuv = (const u16*)d_in[9];
  const u16* woB = (const u16*)d_in[10];
  char* ws = (char*)d_ws;
  hipFuncSetAttribute((const void*)k2_attn, hipFuncAttributeMaxDynamicSharedMemorySize, K2_LDS);
  k1_proj<<<256, 512, 0, stream>>>(xA, wqA, xB, wqB, wuk, ws);
  k2_attn<<<256, 256, K2_LDS, stream>>>(kc, vc, ckv, ws);
  k3_tail<<<512, 256, 0, stream>>>(wuv, woA, woB, ws, (u16*)d_out);
}
```
